```python
import torch

def _softmax(x, axis=-1):
    return torch.softmax(x, dim=axis)

def _gelu(x):
    return torch.nn.functional.gelu(x, approximate='tanh')

def _silu(x):
    return torch.nn.functional.silu(x)

def _sigmoid(x):
    return torch.sigmoid(x)

def _layernorm(x, axis=-1, eps=1e-05):
    m = x.mean(dim=axis, keepdim=True)
    v = x.var(dim=axis, keepdim=True, unbiased=False)
    return (x - m) / torch.sqrt(v + eps)

def _scatter_add(tgt, idx, src, axis=0):
    return tgt.clone().index_add_(axis, idx, src)

def _segment_sum(src, seg, n):
    out = torch.zeros((n,) + src.shape[1:], dtype=src.dtype)
    return out.index_add_(0, seg, src)

def _pad(x, pw):
    flat = tuple((v for pair in reversed(pw) for v in pair))
    return torch.nn.functional.pad(x, flat)
kernel_name = 'concat_heads__memory_aug_recurrent_mem_tr__sliding_local_dilated__prompt'

def setup_inputs():

    _w = lambda a: (lambda t: t.to(torch.bfloat16) if t.is_floating_point() else t)(torch.as_tensor(a))

    def _A_setup():
        B, S, Dm = (1, 2048, 640)
        H, Dh = (32, 128)
        M, seg = (8, 32)
        seg = min(seg, S)
        S = S // seg * seg
        return {'x': _w((lambda *s: torch.randn(*s, dtype=torch.bfloat16) * 0.05)(B, S, Dm)), 'mem0': _w((lambda *s: torch.randn(*s, dtype=torch.bfloat16) * 1.6)(B, M, Dm)), 'Wq': _w((lambda *s: torch.randn(*s, dtype=torch.bfloat16) * 0.8)(Dm, H, Dh) * Dm ** (-0.5)), 'Wk': _w((lambda *s: torch.randn(*s, dtype=torch.bfloat16) * 0.8)(Dm, H, Dh) * Dm ** (-0.5)), 'Wv': _w((lambda *s: torch.randn(*s, dtype=torch.bfloat16) * 1.6)(Dm, H, Dh) * Dm ** (-0.5)), 'Wo': _w((lambda *s: torch.randn(*s, dtype=torch.bfloat16) * 1.6)(H, Dh, Dm) * (H * Dh) ** (-0.5)), 'seg_len': seg}
    _A_in = _A_setup()

    def _B_setup():
        B, S, D = (1, 2048, 640)
        return {'x': _w((lambda *s: torch.randn(*s, dtype=torch.bfloat16) * 0.2)(B, S, D)), 'wqkv': _w((lambda *s: torch.randn(*s, dtype=torch.bfloat16) * 0.2)(D, 3 * D)), 'wo': _w((lambda *s: torch.randn(*s, dtype=torch.bfloat16) * 0.05)(D, D))}
    _B_in = _B_setup()
    return {'A_x': _A_in['x'], 'A_mem0': _A_in['mem0'], 'A_Wq': _A_in['Wq'], 'A_Wk': _A_in['Wk'], 'A_Wv': _A_in['Wv'], 'A_Wo': _A_in['Wo'], 'A_seg_len': _A_in['seg_len'], 'B_x': _B_in['x'], 'B_wqkv': _B_in['wqkv'], 'B_wo': _B_in['wo']}

def reference(A_x, A_mem0, A_Wq, A_Wk, A_Wv, A_Wo, A_seg_len, B_x, B_wqkv, B_wo):

    def _A_ref(x, mem0, Wq, Wk, Wv, Wo, seg_len):
        B, S, Dm = x.shape
        H, Dh = (Wq.shape[1], Wq.shape[2])
        M = mem0.shape[1]
        scale = Dh ** (-0.5)
        n_seg = S // seg_len
        L = M + seg_len
        idx = torch.arange(L)
        mask = (idx[:, None] < M) | (idx[:, None] >= idx[None, :])
        mem = mem0
        outs = []
        for s in range(n_seg):
            xs = x[:, s * seg_len:(s + 1) * seg_len, :].clone()
            h = torch.concatenate([mem, xs], axis=1)
            q = torch.einsum('btd,dhe->bhte', h, Wq)
            k = torch.einsum('btd,dhe->bhte', h, Wk)
            v = torch.einsum('btd,dhe->bhte', h, Wv)
            scores = torch.einsum('bhqe,bhke->bhqk', q, k) * scale
            scores = torch.where(mask[None, None], scores, -1000000000.0)
            p = _softmax(scores, axis=-1)
            o = torch.einsum('bhqk,bhke->bhqe', p, v)
            o = torch.einsum('bhte,hed->btd', o, Wo)
            mem = o[:, :M, :].clone()
            outs.append(o[:, M:, :])
        return torch.concatenate(outs, axis=1)

    def _B_ref(x, wqkv, wo):
        B, S, D = x.shape
        H = 32
        Dh = D // H
        W, dil = (8, 2)
        qkv = (lambda x, *d: x.permute(*d))((x @ wqkv).reshape(B, S, 3, H, Dh), 2, 0, 3, 1, 4)
        q, k, v = (qkv[0], qkv[1], qkv[2])
        scale = Dh ** (-0.5)
        pos = torch.arange(S)
        off = torch.arange(W) * dil
        idx = pos[:, None] - off[None, :]
        mask = idx >= 0
        idx_c = torch.where(mask, idx, 0).reshape(-1)
        kg = k[:, :, idx_c, :].reshape(B, H, S, W, Dh)
        vg = v[:, :, idx_c, :].reshape(B, H, S, W, Dh)
        logits = torch.einsum('bhsd,bhswd->bhsw', q, kg) * scale
        logits = torch.where(mask[None, None, :, :], logits, -1000000000.0)
        attn = _softmax(logits, axis=-1)
        o = torch.einsum('bhsw,bhswd->bhsd', attn, vg)
        return (lambda x, *d: x.permute(*d))(o, 0, 2, 1, 3).reshape(B, S, D) @ wo
    a = _A_ref(A_x, A_mem0, A_Wq, A_Wk, A_Wv, A_Wo, A_seg_len)
    b = _B_ref(B_x, B_wqkv, B_wo)
    return torch.concatenate([a, b], axis=-1)
```

```cpp
#include <hip/hip_runtime.h>
#include <hip/hip_bf16.h>
#include <stdint.h>

typedef unsigned short u16;
typedef short bf16x8 __attribute__((ext_vector_type(8)));
typedef short s16x4 __attribute__((ext_vector_type(4)));
typedef float f32x4 __attribute__((ext_vector_type(4)));
typedef unsigned v4u __attribute__((ext_vector_type(4)));
typedef unsigned v2u __attribute__((ext_vector_type(2)));
#define LAS __attribute__((address_space(3)))
#define DI __device__ __forceinline__

constexpr int SLEN = 2048, DM = 640, NH = 32, DH = 128, HD = 4096, NSEG = 64;
constexpr int OUTW = 1280;
constexpr size_t OFF_FLAGKV = 0;
constexpr size_t OFF_FLAGM  = 8192;
constexpr size_t OFF_CNTOX  = 16384;
constexpr size_t OFF_CNTB   = 20480;
constexpr size_t OFF_MEMBUF = 24576;
constexpr size_t OFF_OBUF   = OFF_MEMBUF + 64 * 10240;
constexpr size_t POISON_BYTES = OFF_OBUF + (size_t)64 * 65536;
constexpr size_t OFF_KX  = (size_t)8 << 20;
constexpr size_t OFF_VXT = (size_t)24 << 20;
constexpr size_t OFF_QX  = (size_t)40 << 20;
constexpr size_t OFF_KM  = (size_t)56 << 20;
constexpr size_t OFF_VMT = (size_t)60 << 20;
constexpr size_t OFF_OX  = (size_t)64 << 20;
constexpr size_t OFF_OB  = (size_t)80 << 20;
constexpr unsigned SENT = 0xFFFFFFFFu;
constexpr int SPIN_CAP = 300000;
constexpr int LDS_BYTES = 160 * 1024;

#define STAMP(s, i) do {} while (0)
DI float bf2f(u16 b) { return __builtin_bit_cast(float, (unsigned)b << 16); }
DI u16 f2bf(float f) { __bf16 b = (__bf16)f; return __builtin_bit_cast(u16, b); }
DI float rbf(float f) { return bf2f(f2bf(f)); }
DI unsigned pack2(float a, float b) { return (unsigned)f2bf(a) | ((unsigned)f2bf(b) << 16); }
DI unsigned unsent(unsigned x) { return x == SENT ? 0xFFFEFFFFu : x; }
DI bool valid4(v4u v) { return v.x != SENT && v.y != SENT && v.z != SENT && v.w != SENT; }
DI f32x4 mfma16(bf16x8 a, bf16x8 b, f32x4 c) { return __builtin_amdgcn_mfma_f32_16x16x32_bf16(a, b, c, 0, 0, 0); }
DI bf16x8 as_frag(v4u v) { return __builtin_bit_cast(bf16x8, v); }
DI s16x4 tr_read(const char* p) {
  typedef short v4i16_t __attribute__((ext_vector_type(4)));
  return __builtin_amdgcn_ds_read_tr16_b64_v4i16((LAS v4i16_t*)(p));
}
DI float red_max(float x) {
  unsigned u = __builtin_bit_cast(unsigned, x);
  auto r = __builtin_amdgcn_permlane16_swap(u, u, false, false);
  x = fmaxf(__builtin_bit_cast(float, (unsigned)r[0]), __builtin_bit_cast(float, (unsigned)r[1]));
  u = __builtin_bit_cast(unsigned, x);
  auto r2 = __builtin_amdgcn_permlane32_swap(u, u, false, false);
  return fmaxf(__builtin_bit_cast(float, (unsigned)r2[0]), __builtin_bit_cast(float, (unsigned)r2[1]));
}
DI float red_sum(float x) {
  unsigned u = __builtin_bit_cast(unsigned, x);
  auto r = __builtin_amdgcn_permlane16_swap(u, u, false, false);
  x = __builtin_bit_cast(float, (unsigned)r[0]) + __builtin_bit_cast(float, (unsigned)r[1]);
  u = __builtin_bit_cast(unsigned, x);
  auto r2 = __builtin_amdgcn_permlane32_swap(u, u, false, false);
  return __builtin_bit_cast(float, (unsigned)r2[0]) + __builtin_bit_cast(float, (unsigned)r2[1]);
}
DI bf16x8 cat4(s16x4 lo, s16x4 hi) { bf16x8 r; r[0]=lo[0]; r[1]=lo[1]; r[2]=lo[2]; r[3]=lo[3]; r[4]=hi[0]; r[5]=hi[1]; r[6]=hi[2]; r[7]=hi[3]; return r; }
DI bf16x8 lds_frag(const char* p) { return *(const bf16x8*)p; }

DI v4u ld16_sc1(const void* p) { v4u r; asm volatile("global_load_dwordx4 %0, %1, off sc1\n\ts_waitcnt vmcnt(0)" : "=&v"(r) : "v"(p) : "memory"); return r; }
DI void ld16x2_sc1(const void* p0, const void* p1, v4u& a, v4u& b) {
  asm volatile("global_load_dwordx4 %0, %2, off sc1\n\tglobal_load_dwordx4 %1, %3, off sc1\n\ts_waitcnt vmcnt(0)"
               : "=&v"(a), "=&v"(b) : "v"(p0), "v"(p1) : "memory");
}
DI unsigned ld4_sc1(const void* p) { unsigned r; asm volatile("global_load_dword %0, %1, off sc1\n\ts_waitcnt vmcnt(0)" : "=&v"(r) : "v"(p) : "memory"); return r; }
DI void st16_sc1(void* p, v4u v) { asm volatile("global_store_dwordx4 %0, %1, off sc1\n\ts_nop 1" :: "v"(p), "v"(v) : "memory"); }
DI void st8_sc1(void* p, v2u v) { asm volatile("global_store_dwordx2 %0, %1, off sc1\n\ts_nop 1" :: "v"(p), "v"(v) : "memory"); }
DI void st4_sc1(void* p, unsigned v) { asm volatile("global_store_dword %0, %1, off sc1\n\ts_nop 1" :: "v"(p), "v"(v) : "memory"); }
DI float dpp_xor1(float x) { return __builtin_bit_cast(float, __builtin_amdgcn_update_dpp(0, __builtin_bit_cast(int, x), 0xB1, 0xf, 0xf, true)); }
DI float dpp_xor2(float x) { return __builtin_bit_cast(float, __builtin_amdgcn_update_dpp(0, __builtin_bit_cast(int, x), 0x4E, 0xf, 0xf, true)); }
DI float dpp_shl4(float x) { return __builtin_bit_cast(float, __builtin_amdgcn_update_dpp(0, __builtin_bit_cast(int, x), 0x104, 0xf, 0xf, true)); }
DI void drain() { asm volatile("s_waitcnt vmcnt(0)" ::: "memory"); }
DI void ld16x16_sc1(const char* p, v4u (&a)[16]) {
  asm volatile(
    "global_load_dwordx4 %0, %16, off sc1\n\t"
    "global_load_dwordx4 %1, %16, off offset:64 sc1\n\t"
    "global_load_dwordx4 %2, %16, off offset:128 sc1\n\t"
    "global_load_dwordx4 %3, %16, off offset:192 sc1\n\t"
    "global_load_dwordx4 %4, %16, off offset:256 sc1\n\t"
    "global_load_dwordx4 %5, %16, off offset:320 sc1\n\t"
    "global_load_dwordx4 %6, %16, off offset:384 sc1\n\t"
    "global_load_dwordx4 %7, %16, off offset:448 sc1\n\t"
    "global_load_dwordx4 %8, %16, off offset:512 sc1\n\t"
    "global_load_dwordx4 %9, %16, off offset:576 sc1\n\t"
    "global_load_dwordx4 %10, %16, off offset:640 sc1\n\t"
    "global_load_dwordx4 %11, %16, off offset:704 sc1\n\t"
    "global_load_dwordx4 %12, %16, off offset:768 sc1\n\t"
    "global_load_dwordx4 %13, %16, off offset:832 sc1\n\t"
    "global_load_dwordx4 %14, %16, off offset:896 sc1\n\t"
    "global_load_dwordx4 %15, %16, off offset:960 sc1\n\t"
    "s_waitcnt vmcnt(0)"
    : "=&v"(a[0]), "=&v"(a[1]), "=&v"(a[2]), "=&v"(a[3]), "=&v"(a[4]), "=&v"(a[5]), "=&v"(a[6]), "=&v"(a[7]),
      "=&v"(a[8]), "=&v"(a[9]), "=&v"(a[10]), "=&v"(a[11]), "=&v"(a[12]), "=&v"(a[13]), "=&v"(a[14]), "=&v"(a[15])
    : "v"(p) : "memory");
}
DI void ld16x8s512_sc1(const char* p, v4u (&a)[8]) {
  asm volatile(
    "global_load_dwordx4 %0, %8, off sc1\n\t"
    "global_load_dwordx4 %1, %8, off offset:512 sc1\n\t"
    "global_load_dwordx4 %2, %8, off offset:1024 sc1\n\t"
    "global_load_dwordx4 %3, %8, off offset:1536 sc1\n\t"
    "global_load_dwordx4 %4, %8, off offset:2048 sc1\n\t"
    "global_load_dwordx4 %5, %8, off offset:2560 sc1\n\t"
    "global_load_dwordx4 %6, %8, off offset:3072 sc1\n\t"
    "global_load_dwordx4 %7, %8, off offset:3584 sc1\n\t"
    "s_waitcnt vmcnt(0)"
    : "=&v"(a[0]), "=&v"(a[1]), "=&v"(a[2]), "=&v"(a[3]), "=&v"(a[4]), "=&v"(a[5]), "=&v"(a[6]), "=&v"(a[7])
    : "v"(p) : "memory");
}
DI void ld16x10_sc1(const char* p, v4u (&a)[10]) {
  asm volatile(
    "global_load_dwordx4 %0, %10, off sc1\n\t"
    "global_load_dwordx4 %1, %10, off offset:64 sc1\n\t"
    "global_load_dwordx4 %2, %10, off offset:128 sc1\n\t"
    "global_load_dwordx4 %3, %10, off offset:192 sc1\n\t"
    "global_load_dwordx4 %4, %10, off offset:256 sc1\n\t"
    "global_load_dwordx4 %5, %10, off offset:320 sc1\n\t"
    "global_load_dwordx4 %6, %10, off offset:384 sc1\n\t"
    "global_load_dwordx4 %7, %10, off offset:448 sc1\n\t"
    "global_load_dwordx4 %8, %10, off offset:512 sc1\n\t"
    "global_load_dwordx4 %9, %10, off offset:576 sc1\n\t"
    "s_waitcnt vmcnt(0)"
    : "=&v"(a[0]), "=&v"(a[1]), "=&v"(a[2]), "=&v"(a[3]), "=&v"(a[4]), "=&v"(a[5]), "=&v"(a[6]), "=&v"(a[7]),
      "=&v"(a[8]), "=&v"(a[9])
    : "v"(p) : "memory");
}
DI void wait_word(const unsigned* p, unsigned want, int& dead) {
  if ((threadIdx.x >> 6) == 0 && !dead) {
    int sp = 0;
    for (; sp < SPIN_CAP; ++sp) {
      unsigned v = ld4_sc1(p);
      if (__builtin_amdgcn_readfirstlane(v) == want) break;
      __builtin_amdgcn_s_sleep(8);
    }
    if (sp >= SPIN_CAP) dead = 1;
  }
  __syncthreads();
}

constexpr int AST = 1296;
constexpr int KST = 272;
constexpr int VST = 136;
constexpr int L_WRES = 0;
constexpr int L_STG = 98304;
constexpr int STG_BUF = 3 * 32 * KST;
constexpr int S1_A = 98304;
constexpr int S1_K = S1_A + 10496;
constexpr int S1_VT = S1_K + 48 * KST;
constexpr int S1_Q = S1_VT + 128 * VST;
constexpr int S4_A = 98304;
constexpr int S4_T = S4_A + 2 * 16 * AST;
constexpr int TST = 528;

DI void load_w3(const u16* __restrict__ Wq, const u16* __restrict__ Wk, const u16* __restrict__ Wv, int h,
                char* smem, bf16x8 (&wr)[48], int t) {
  const int lane = t & 63, w = t >> 6;
  const int r = t >> 4, c16 = t & 15;
  const size_t goff = (size_t)r * HD + h * DH + c16 * 8;
  const int g = lane >> 4, qq = (lane & 15) >> 2, p = lane & 3;
  const int troff = (8 * g + qq) * KST + (16 * w + 4 * p) * 2;
  v4u pf[4][3];
#pragma unroll
  for (int i = 0; i < 4; ++i) {
    pf[i][0] = *(const v4u*)(Wq + goff + (size_t)i * 32 * HD);
    pf[i][1] = *(const v4u*)(Wk + goff + (size_t)i * 32 * HD);
    pf[i][2] = *(const v4u*)(Wv + goff + (size_t)i * 32 * HD);
  }
#pragma unroll
  for (int ks = 0; ks < 20; ++ks) {
    char* stg = smem + L_STG + (ks & 1) * STG_BUF;
#pragma unroll
    for (int m = 0; m < 3; ++m) *(v4u*)(stg + m * 32 * KST + r * KST + c16 * 16) = pf[ks & 3][m];
    if (ks + 4 < 20) {
      pf[ks & 3][0] = *(const v4u*)(Wq + goff + (size_t)(ks + 4) * 32 * HD);
      pf[ks & 3][1] = *(const v4u*)(Wk + goff + (size_t)(ks + 4) * 32 * HD);
      pf[ks & 3][2] = *(const v4u*)(Wv + goff + (size_t)(ks + 4) * 32 * HD);
    }
    __syncthreads();
#pragma unroll
    for (int m = 0; m < 3; ++m) {
      s16x4 lo = tr_read(stg + m * 32 * KST + troff);
      s16x4 hi = tr_read(stg + m * 32 * KST + troff + 4 * KST);
      bf16x8 f = cat4(lo, hi);
      if (ks < 16) wr[ks * 3 + m] = f;
      else *(bf16x8*)(smem + L_WRES + w * 12288 + ((ks - 16) * 3 + m) * 1024 + lane * 16) = f;
    }
  }
  __syncthreads();
}

DI void proj3(const char* ldsA, int arow, const char* smem, const bf16x8 (&wr)[48], int t, f32x4& aq, f32x4& ak, f32x4& av) {
  const int lane = t & 63, w = t >> 6, q = lane >> 4;
  const char* ap = ldsA + arow * AST + q * 16;
  int woff = w * 12288 + lane * 16;
  asm volatile("" : "+v"(woff));
  const char* wp = smem + L_WRES + woff;
  aq = (f32x4){0.f, 0.f, 0.f, 0.f}; ak = aq; av = aq;
#pragma unroll
  for (int ks = 0; ks < 20; ++ks) {
    bf16x8 a = lds_frag(ap + ks * 64);
    bf16x8 b0, b1, b2;
    if (ks < 16) { b0 = wr[ks * 3]; b1 = wr[ks * 3 + 1]; b2 = wr[ks * 3 + 2]; }
    else { b0 = lds_frag(wp + ((ks - 16) * 3) * 1024); b1 = lds_frag(wp + ((ks - 16) * 3 + 1) * 1024); b2 = lds_frag(wp + ((ks - 16) * 3 + 2) * 1024); }
    aq = mfma16(a, b0, aq);
    ak = mfma16(a, b1, ak);
    av = mfma16(a, b2, av);
    if ((ks & 3) == 3) { asm volatile("" : "+v"(aq), "+v"(ak), "+v"(av)); __builtin_amdgcn_sched_barrier(0); }
  }
}

template <class VIS>
DI void softmax_pt(const f32x4 (&st)[3], float scale, VIS vis, bf16x8& pb0, bf16x8& pb1) {
  float s[3][4]; float mx = -3.0e38f;
#pragma unroll
  for (int kt = 0; kt < 3; ++kt)
#pragma unroll
    for (int r = 0; r < 4; ++r) {
      float v = rbf(rbf(st[kt][r]) * scale);
      s[kt][r] = v;
      if (vis(kt, r)) mx = fmaxf(mx, v);
    }
  mx = red_max(mx);
  float sum = 0.f;
#pragma unroll
  for (int kt = 0; kt < 3; ++kt)
#pragma unroll
    for (int r = 0; r < 4; ++r) {
      float e = vis(kt, r) ? __expf(s[kt][r] - mx) : 0.f;
      s[kt][r] = e; sum += e;
    }
  sum = red_sum(sum);
  const float inv = 1.0f / sum;
#pragma unroll
  for (int r = 0; r < 4; ++r) {
    pb0[r] = (short)f2bf(s[0][r] * inv);
    pb0[4 + r] = (short)f2bf(s[1][r] * inv);
    pb1[r] = (short)f2bf(s[2][r] * inv);
    pb1[4 + r] = 0;
  }
}

struct Params {
  const u16* ax; const u16* mem0; const u16* Wq; const u16* Wk; const u16* Wv; const u16* Wo;
  const u16* bx; const u16* wqkv; const u16* wo;
  u16* out; char* ws;
};

DI void role_s1(const Params& P, char* smem, int h) {
  int t = threadIdx.x;
  bf16x8 wr[48];
  load_w3(P.Wq, P.Wk, P.Wv, h, smem, wr, t);
  char* ws = P.ws;
  int dead = 0;
  for (int i = t; i < 8 * 16; i += 512) *(v4u*)(smem + S1_K + (40 + (i >> 4)) * KST + (i & 15) * 16) = (v4u){0, 0, 0, 0};
  for (int i = t; i < 128 * 6; i += 512) *(v2u*)(smem + S1_VT + (i / 6) * VST + 80 + (i % 6) * 8) = (v2u){0, 0};
  const float scale = 0.08838834764831845f;
  for (int s = 0; s < NSEG; ++s) {
    asm volatile("" : "+v"(t));
    const int lane = t & 63, w = t >> 6, q = lane >> 4, l15 = lane & 15;
    {
      wait_word((const unsigned*)(ws + OFF_FLAGKV) + h * 64 + s, 1u, dead);
      const char* kx = ws + OFF_KX + ((size_t)(s * NH + h)) * 8192;
      const char* vx = ws + OFF_VXT + ((size_t)(s * NH + h)) * 8192;
      v4u a, b;
      ld16x2_sc1(kx + t * 16, vx + t * 16, a, b);
      *(v4u*)(smem + S1_K + (8 + (t >> 4)) * KST + (t & 15) * 16) = a;
      v2u b0 = {b.x, b.y}, b1 = {b.z, b.w};
      *(v2u*)(smem + S1_VT + (t >> 2) * VST + 16 + (t & 3) * 16) = b0;
      *(v2u*)(smem + S1_VT + (t >> 2) * VST + 16 + (t & 3) * 16 + 8) = b1;
    }
    STAMP(s, 0);
    {
      const int c0 = t, c1 = 512 + t;
      v4u a, b = {0, 0, 0, 0};
      if (s == 0) {
        a = *(const v4u*)((const char*)P.mem0 + c0 * 16);
        if (w < 2) b = *(const v4u*)((const char*)P.mem0 + c1 * 16);
      } else {
        const char* src = ws + OFF_MEMBUF + (size_t)s * 10240;
        for (int sp = 0;; ++sp) {
          bool ok;
          if (w < 2) { ld16x2_sc1(src + c0 * 16, src + c1 * 16, a, b); ok = valid4(a) && valid4(b); }
          else { a = ld16_sc1(src + c0 * 16); ok = valid4(a); }
          if (__all(ok)) break;
          if (dead || sp > SPIN_CAP) { dead = 1; break; }
        }
      }
      *(v4u*)(smem + S1_A + (c0 / 80) * AST + (c0 % 80) * 16) = a;
      if (w < 2) *(v4u*)(smem + S1_A + (c1 / 80) * AST + (c1 % 80) * 16) = b;
    }
    __syncthreads();
    STAMP(s, 1);
    f32x4 aq, ak, av;
    proj3(smem + S1_A, lane & 7, smem, wr, t, aq, ak, av);
    if (q < 2) {
#pragma unroll
      for (int r = 0; r < 4; ++r) {
        *(u16*)(smem + S1_Q + (4 * q + r) * KST + (16 * w + l15) * 2) = f2bf(aq[r]);
        *(u16*)(smem + S1_K + (4 * q + r) * KST + (16 * w + l15) * 2) = f2bf(ak[r]);
      }
      v2u vv = {pack2(av[0], av[1]), pack2(av[2], av[3])};
      *(v2u*)(smem + S1_VT + (16 * w + l15) * VST + q * 8) = vv;
    }
    __syncthreads();
    STAMP(s, 2);
    if (s < NSEG - 1 && w < 4) {
      const char* qp = smem + S1_Q + (lane & 7) * KST + q * 16;
      const char* kp = smem + S1_K + l15 * KST + q * 16;
      f32x4 st[3];
      st[0] = (f32x4){0.f, 0.f, 0.f, 0.f}; st[1] = st[0]; st[2] = st[0];
      bf16x8 qf0 = lds_frag(qp), ka0 = lds_frag(kp), kb0 = lds_frag(kp + 16 * KST), kc0 = lds_frag(kp + 32 * KST);
      bf16x8 qf1 = lds_frag(qp + 64), ka1 = lds_frag(kp + 64), kb1 = lds_frag(kp + 16 * KST + 64), kc1 = lds_frag(kp + 32 * KST + 64);
      st[0] = mfma16(ka0, qf0, st[0]); st[1] = mfma16(kb0, qf0, st[1]); st[2] = mfma16(kc0, qf0, st[2]);
      qf0 = lds_frag(qp + 128); ka0 = lds_frag(kp + 128); kb0 = lds_frag(kp + 16 * KST + 128); kc0 = lds_frag(kp + 32 * KST + 128);
      st[0] = mfma16(ka1, qf1, st[0]); st[1] = mfma16(kb1, qf1, st[1]); st[2] = mfma16(kc1, qf1, st[2]);
      qf1 = lds_frag(qp + 192); ka1 = lds_frag(kp + 192); kb1 = lds_frag(kp + 16 * KST + 192); kc1 = lds_frag(kp + 32 * KST + 192);
      st[0] = mfma16(ka0, qf0, st[0]); st[1] = mfma16(kb0, qf0, st[1]); st[2] = mfma16(kc0, qf0, st[2]);
      const char* vp = smem + S1_VT + (32 * w + l15) * VST + q * 8;
      s16x4 v0 = *(const s16x4*)(vp), v1 = *(const s16x4*)(vp + 32), v2 = *(const s16x4*)(vp + 64);
      s16x4 u0 = *(const s16x4*)(vp + 16 * VST), u1 = *(const s16x4*)(vp + 16 * VST + 32), u2 = *(const s16x4*)(vp + 16 * VST + 64);
      st[0] = mfma16(ka1, qf1, st[0]); st[1] = mfma16(kb1, qf1, st[1]); st[2] = mfma16(kc1, qf1, st[2]);
      bf16x8 pb0, pb1;
      softmax_pt(st, scale, [&](int kt, int r) { return kt < 2 || q < 2; }, pb0, pb1);
      const s16x4 z4 = {0, 0, 0, 0};
      f32x4 o = {0.f, 0.f, 0.f, 0.f}, o2 = o;
      o = mfma16(cat4(v0, v1), pb0, o);
      o2 = mfma16(cat4(u0, u1), pb0, o2);
      o = mfma16(cat4(v2, z4), pb1, o);
      o2 = mfma16(cat4(u2, z4), pb1, o2);
      if (l15 < 8) {
        v2u ov = {unsent(pack2(o[0], o[1])), unsent(pack2(o[2], o[3]))};
        v2u ov2 = {unsent(pack2(o2[0], o2[1])), unsent(pack2(o2[2], o2[3]))};
        char* dst = ws + OFF_OBUF + (size_t)s * 65536 + h * 2048 + (2 * w) * 256 + l15 * 32 + q * 8;
        st8_sc1(dst, ov);
        st8_sc1(dst + 256, ov2);
      }
    }
    STAMP(s, 3);
    if (t < 128) {
      v4u kv = *(const v4u*)(smem + S1_K + (t >> 4) * KST + (t & 15) * 16);
      st16_sc1(ws + OFF_KM + ((size_t)(s * NH + h)) * 2048 + t * 16, kv);
    } else if (t < 256) {
      const int d = t - 128;
      v2u a = *(const v2u*)(smem + S1_VT + d * VST), b = *(const v2u*)(smem + S1_VT + d * VST + 8);
      v4u vv = {a.x, a.y, b.x, b.y};
      st16_sc1(ws + OFF_VMT + ((size_t)(s * NH + h)) * 2048 + d * 16, vv);
    }
    drain();
    __syncthreads();
    if (t == 0) st4_sc1((unsigned*)(ws + OFF_FLAGM) + h * 64 + s, 1u);
    STAMP(s, 4);
  }
}

DI void load_wo16(const u16* __restrict__ Wo, int c, char* smem, bf16x8 (&wb)[16], int t) {
  const int lane = t & 63, w = t >> 6;
  const u16* src = Wo + (size_t)(512 * w + (lane >> 1)) * DM + 16 * c + 8 * (lane & 1);
  v4u tmp[16];
#pragma unroll
  for (int ks = 0; ks < 16; ++ks) tmp[ks] = *(const v4u*)(src + (size_t)ks * 32 * DM);
  char* base = smem + w * 16384;
#pragma unroll
  for (int ks = 0; ks < 16; ++ks) *(v4u*)(base + ks * 1024 + lane * 16) = tmp[ks];
  __syncthreads();
  const int g = lane >> 4, qq = (lane & 15) >> 2, p = lane & 3;
#pragma unroll
  for (int ks = 0; ks < 16; ++ks) {
    s16x4 lo = tr_read(base + ks * 1024 + (8 * g + qq) * 32 + p * 8);
    s16x4 hi = tr_read(base + ks * 1024 + (8 * g + 4 + qq) * 32 + p * 8);
    wb[ks] = cat4(lo, hi);
  }
  __syncthreads();
}

DI void role_s2(const Params& P, char* smem, int c) {
  const int t = threadIdx.x, lane = t & 63, w = t >> 6, q = lane >> 4, l15 = lane & 15;
  bf16x8 wb[16];
  load_wo16(P.Wo, c, smem, wb, t);
  char* ws = P.ws;
  int dead = 0;
  float* red = (float*)smem;
  for (int s = 0; s < NSEG - 1; ++s) {
    v4u a[8];
    const char* src = ws + OFF_OBUF + (size_t)s * 65536 + w * 8192 + (l15 >> 3) * 4096 + (q >> 1) * 256 + (l15 & 7) * 32 + (q & 1) * 16;
    const char* prb = ws + OFF_OBUF + (size_t)s * 65536 + w * 8192 + (lane & 31) * 256 + 252;
    for (int sp = 0;; ++sp) {
      unsigned pv = ld4_sc1(prb);
      if (__any(pv != SENT)) break;
      if (dead || sp > SPIN_CAP) { dead = 1; break; }
    }
    for (int sp = 0;; ++sp) {
      bool ok = true;
      ld16x8s512_sc1(src, a);
#pragma unroll
      for (int i = 0; i < 8; ++i) ok = ok && valid4(a[i]);
      if (__all(ok)) break;
      if (dead || sp > SPIN_CAP) { dead = 1; break; }
    }
    STAMP(s, 0);
    f32x4 acc0 = {0.f, 0.f, 0.f, 0.f}, acc1 = acc0;
#pragma unroll
    for (int i = 0; i < 8; ++i) {
      acc0 = mfma16(as_frag(a[i]), wb[i], acc0);
      acc1 = mfma16(as_frag(a[i]), wb[i + 8], acc1);
    }
    float* rb = red + (s & 1) * 2048 + w * 256 + (q >> 1) * 128;
#pragma unroll
    for (int r = 0; r < 4; ++r) rb[(4 * (q & 1) + r) * 16 + l15] = (q < 2) ? acc0[r] : acc1[r];
    __syncthreads();
    STAMP(s, 1);
    {
      const int o = t >> 2, pt = t & 3;
      const float* rp = red + (s & 1) * 2048 + pt * 512 + o;
      float x = (rp[0] + rp[128]) + (rp[256] + rp[384]);
      x += dpp_xor1(x);
      x += dpp_xor2(x);
      const float xn = dpp_shl4(x);
      if ((t & 7) == 0) {
        const int row = o >> 4, col = o & 15;
        st4_sc1(ws + OFF_MEMBUF + (size_t)(s + 1) * 10240 + row * 1280 + (16 * c + col) * 2, unsent(pack2(x, xn)));
      }
    }
    STAMP(s, 2);
  }
}

DI void role_s4(const Params& P, char* smem, int h, int par) {
  const int t = threadIdx.x, lane = t & 63, w = t >> 6, q = lane >> 4, l15 = lane & 15;
  char* ws = P.ws;
  int dead = 0;
  {
    bf16x8 wr[48];
    load_w3(P.Wq, P.Wk, P.Wv, h, smem, wr, t);
    v4u pa[3];
    auto fetchA = [&](int j) {
      const int row0 = (2 * (j >> 1) + par) * 32 + (j & 1) * 16;
      const char* src = (const char*)P.ax + (size_t)row0 * (DM * 2);
      pa[0] = *(const v4u*)(src + t * 16);
      pa[1] = *(const v4u*)(src + (512 + t) * 16);
      if (t < 256) pa[2] = *(const v4u*)(src + (1024 + t) * 16);
    };
    auto putA = [&](int buf) {
      char* dst = smem + S4_A + buf * 16 * AST;
      *(v4u*)(dst + (t / 80) * AST + (t % 80) * 16) = pa[0];
      *(v4u*)(dst + ((512 + t) / 80) * AST + ((512 + t) % 80) * 16) = pa[1];
      if (t < 256) *(v4u*)(dst + ((1024 + t) / 80) * AST + ((1024 + t) % 80) * 16) = pa[2];
    };
    fetchA(0); putA(0); fetchA(1);
    __syncthreads();
    for (int j = 0; j < 64; ++j) {
      const int seg = 2 * (j >> 1) + par, mt = j & 1;
      f32x4 aq, ak, av;
      proj3(smem + S4_A + (j & 1) * 16 * AST, l15, smem, wr, t, aq, ak, av);
      char* T = smem + S4_T + (j & 1) * 16 * TST;
#pragma unroll
      for (int r = 0; r < 4; ++r) {
        *(u16*)(T + (4 * q + r) * TST + (16 * w + l15) * 2) = f2bf(aq[r]);
        *(u16*)(T + (4 * q + r) * TST + 256 + (16 * w + l15) * 2) = f2bf(ak[r]);
      }
      {
        v2u vv = {pack2(av[0], av[1]), pack2(av[2], av[3])};
        st8_sc1(ws + OFF_VXT + ((size_t)(seg * NH + h)) * 8192 + (16 * w + l15) * 64 + (16 * mt + 4 * q) * 2, vv);
      }
      if (j + 1 < 64) { putA((j + 1) & 1); if (j + 2 < 64) fetchA(j + 2); }
      __syncthreads();
      {
        const int row = t >> 5, ch = t & 31;
        v4u v = *(const v4u*)(T + row * TST + ch * 16);
        const size_t o = ((size_t)(seg * NH + h)) * 8192 + (16 * mt + row) * 256 + (ch & 15) * 16;
        if (ch < 16) *(v4u*)(ws + OFF_QX + o) = v; else st16_sc1(ws + OFF_KX + o, v);
      }
      if (mt == 1) {
        drain();
        __syncthreads();
        if (t == 0) st4_sc1((unsigned*)(ws + OFF_FLAGKV) + h * 64 + seg, 1u);
        STAMP(j >> 1, 0);
      }
    }
  }
  const float scale = 0.08838834764831845f;
  const int qt = w & 1, dq = w >> 1;
  for (int i = 0; i < 32; ++i) {
    const int seg = 2 * i + par;
    wait_word((const unsigned*)(ws + OFF_FLAGM) + h * 64 + seg, 1u, dead);
    const size_t sh = (size_t)(seg * NH + h);
    const char* km = ws + OFF_KM + sh * 2048;
    const char* vm = ws + OFF_VMT + sh * 2048;
    const char* kx = ws + OFF_KX + sh * 8192;
    const char* vx = ws + OFF_VXT + sh * 8192;
    const char* qx = ws + OFF_QX + sh * 8192;
    v4u kf[3][4], qf[4];
    {
      const char* p0 = (l15 < 8) ? (km + l15 * 256) : (kx + (l15 - 8) * 256);
      const char* p1 = kx + (8 + l15) * 256;
      const char* p2 = kx + (24 + (l15 & 7)) * 256;
      const char* pq = qx + (16 * qt + l15) * 256;
      v4u tmp[16];
      asm volatile(
        "global_load_dwordx4 %0, %16, off sc1\n\t"
        "global_load_dwordx4 %1, %16, off offset:64 sc1\n\t"
        "global_load_dwordx4 %2, %16, off offset:128 sc1\n\t"
        "global_load_dwordx4 %3, %16, off offset:192 sc1\n\t"
        "global_load_dwordx4 %4, %17, off sc1\n\t"
        "global_load_dwordx4 %5, %17, off offset:64 sc1\n\t"
        "global_load_dwordx4 %6, %17, off offset:128 sc1\n\t"
        "global_load_dwordx4 %7, %17, off offset:192 sc1\n\t"
        "global_load_dwordx4 %8, %18, off sc1\n\t"
        "global_load_dwordx4 %9, %18, off offset:64 sc1\n\t"
        "global_load_dwordx4 %10, %18, off offset:128 sc1\n\t"
        "global_load_dwordx4 %11, %18, off offset:192 sc1\n\t"
        "global_load_dwordx4 %12, %19, off sc1\n\t"
        "global_load_dwordx4 %13, %19, off offset:64 sc1\n\t"
        "global_load_dwordx4 %14, %19, off offset:128 sc1\n\t"
        "global_load_dwordx4 %15, %19, off offset:192 sc1\n\t"
        "s_waitcnt vmcnt(0)"
        : "=&v"(tmp[0]), "=&v"(tmp[1]), "=&v"(tmp[2]), "=&v"(tmp[3]), "=&v"(tmp[4]), "=&v"(tmp[5]), "=&v"(tmp[6]), "=&v"(tmp[7]),
          "=&v"(tmp[8]), "=&v"(tmp[9]), "=&v"(tmp[10]), "=&v"(tmp[11]), "=&v"(tmp[12]), "=&v"(tmp[13]), "=&v"(tmp[14]), "=&v"(tmp[15])
        : "v"(p0 + q * 16), "v"(p1 + q * 16), "v"(p2 + q * 16), "v"(pq + q * 16) : "memory");
#pragma unroll
      for (int ks = 0; ks < 4; ++ks) { kf[0][ks] = tmp[ks]; kf[1][ks] = tmp[4 + ks]; kf[2][ks] = tmp[8 + ks]; qf[ks] = tmp[12 + ks]; }
    }
    f32x4 st[3];
#pragma unroll
    for (int kt = 0; kt < 3; ++kt) {
      st[kt] = (f32x4){0.f, 0.f, 0.f, 0.f};
#pragma unroll
      for (int ks = 0; ks < 4; ++ks) st[kt] = mfma16(as_frag(kf[kt][ks]), as_frag(qf[ks]), st[kt]);
    }
    const int qi = 16 * qt + l15;
    bf16x8 pb0, pb1;
    softmax_pt(st, scale, [&](int kt, int r) { const int key = 16 * kt + 4 * q + r; return key < 8 || (key < 40 && key - 8 <= qi); }, pb0, pb1);
#pragma unroll
    for (int dd = 0; dd < 2; ++dd) {
      const int d = 16 * (2 * dq + dd) + l15;
      const char* pa = (q < 2) ? (vm + d * 16 + q * 8) : (vx + d * 64 + (4 * q - 8) * 2);
      const char* pb = vx + d * 64 + (8 + 4 * q) * 2;
      const char* pc = vx + d * 64 + (24 + 4 * (q & 1)) * 2;
      v2u a, b, c;
      asm volatile("global_load_dwordx2 %0, %3, off sc1\n\tglobal_load_dwordx2 %1, %4, off sc1\n\tglobal_load_dwordx2 %2, %5, off sc1\n\ts_waitcnt vmcnt(0)"
                   : "=&v"(a), "=&v"(b), "=&v"(c) : "v"(pa), "v"(pb), "v"(pc) : "memory");
      if (q >= 2) c = (v2u){0, 0};
      v4u f0 = {a.x, a.y, b.x, b.y}, f1 = {c.x, c.y, 0, 0};
      f32x4 o = {0.f, 0.f, 0.f, 0.f};
      o = mfma16(as_frag(f0), pb0, o);
      o = mfma16(as_frag(f1), pb1, o);
      v2u ov = {pack2(o[0], o[1]), pack2(o[2], o[3])};
      st8_sc1(ws + OFF_OX + ((size_t)(seg * 32 + qi)) * 8192 + (h * DH + 16 * (2 * dq + dd) + 4 * q) * 2, ov);
    }
    drain();
    __syncthreads();
    if (t == 0) __hip_atomic_fetch_add((unsigned*)(ws + OFF_CNTOX + seg * 64), 1u, __ATOMIC_RELAXED, __HIP_MEMORY_SCOPE_AGENT);
    STAMP(i, 1);
  }
}

DI void role_s3(const Params& P, char* smem, int c, int par) {
  const int t = threadIdx.x, lane = t & 63, w = t >> 6, q = lane >> 4, l15 = lane & 15;
  bf16x8 wb[16];
  load_wo16(P.Wo, c, smem, wb, t);
  char* ws = P.ws;
  int dead = 0;
  float* red = (float*)smem;
  for (int i = 0; i < 32; ++i) {
    const int seg = 2 * i + par;
    wait_word((const unsigned*)(ws + OFF_CNTOX + seg * 64), 31u, dead);
    float* rb = red + (i & 1) * 4096 + w * 512;
#pragma unroll
    for (int mt = 0; mt < 2; ++mt) {
      v4u a[16];
      ld16x16_sc1(ws + OFF_OX + ((size_t)(seg * 32 + 16 * mt + l15)) * 8192 + (512 * w + 8 * q) * 2, a);
      f32x4 acc0 = {0.f, 0.f, 0.f, 0.f}, acc1 = acc0;
#pragma unroll
      for (int ks = 0; ks < 16; ks += 2) {
        acc0 = mfma16(as_frag(a[ks]), wb[ks], acc0);
        acc1 = mfma16(as_frag(a[ks + 1]), wb[ks + 1], acc1);
      }
#pragma unroll
      for (int r = 0; r < 4; ++r) rb[(16 * mt + 4 * q + r) * 16 + l15] = acc0[r] + acc1[r];
    }
    __syncthreads();
    if (t < 256) {
      const int row = t >> 3, cp = t & 7;
      const float* rp = red + (i & 1) * 4096 + row * 16 + 2 * cp;
      float x0 = 0.f, x1 = 0.f;
#pragma unroll
      for (int ww = 0; ww < 8; ++ww) { x0 += rp[ww * 512]; x1 += rp[ww * 512 + 1]; }
      *(unsigned*)(P.out + (size_t)(seg * 32 + row) * OUTW + 16 * c + 2 * cp) = pack2(x0, x1);
    }
    STAMP(i, 0);
  }
}

constexpr int PB_A = 0;
constexpr int PB_R = 2 * 16 * AST;
constexpr int RST = 496;
constexpr int PB_O = PB_R + 3 * 16 * RST;
constexpr int OST = 176;
constexpr int PB_STG = 81920;
DI void role_pb(const Params& P, char* smem, int g, int rb) {
  const int t = threadIdx.x, lane = t & 63, w = t >> 6, q = lane >> 4, l15 = lane & 15;
  char* ws = P.ws;
  int dead = 0;
  const int gg = lane >> 4, qq = (lane & 15) >> 2, pp = lane & 3;
  {
    bf16x8 wb[40];
#pragma unroll
    for (int nn = 0; nn < 2; ++nn) {
      const int n = (2 * w + nn) < 15 ? (2 * w + nn) : 14;
      const int col0 = (n / 5) * DM + 80 * g + 16 * (n % 5);
      const u16* src = P.wqkv + (size_t)(lane >> 1) * 1920 + col0 + 8 * (lane & 1);
#pragma unroll
      for (int half = 0; half < 2; ++half) {
        v4u tmp[10];
#pragma unroll
        for (int i = 0; i < 10; ++i) tmp[i] = *(const v4u*)(src + (size_t)(half * 10 + i) * 32 * 1920);
        char* base = smem + PB_STG + w * 10240;
#pragma unroll
        for (int i = 0; i < 10; ++i) *(v4u*)(base + i * 1024 + lane * 16) = tmp[i];
        __syncthreads();
#pragma unroll
        for (int i = 0; i < 10; ++i) {
          s16x4 lo = tr_read(base + i * 1024 + (8 * gg + qq) * 32 + pp * 8);
          s16x4 hi = tr_read(base + i * 1024 + (8 * gg + 4 + qq) * 32 + pp * 8);
          wb[nn * 20 + half * 10 + i] = cat4(lo, hi);
        }
        __syncthreads();
      }
    }
    v4u pa[3];
    auto fetchA = [&](int j) {
      const int row0 = 512 * rb - 16 + 16 * j;
      if (row0 < 0) { pa[0] = (v4u){0, 0, 0, 0}; pa[1] = pa[0]; pa[2] = pa[0]; return; }
      const char* src = (const char*)P.bx + (size_t)row0 * (DM * 2);
      pa[0] = *(const v4u*)(src + t * 16);
      pa[1] = *(const v4u*)(src + (512 + t) * 16);
      if (t < 256) pa[2] = *(const v4u*)(src + (1024 + t) * 16);
    };
    auto putA = [&](int buf) {
      char* dst = smem + PB_A + buf * 16 * AST;
      *(v4u*)(dst + (t / 80) * AST + (t % 80) * 16) = pa[0];
      *(v4u*)(dst + ((512 + t) / 80) * AST + ((512 + t) % 80) * 16) = pa[1];
      if (t < 256) *(v4u*)(dst + ((1024 + t) / 80) * AST + ((1024 + t) % 80) * 16) = pa[2];
    };
    fetchA(0); putA(0); fetchA(1);
    __syncthreads();
    const float scale = 0.22360679774997896f;
    for (int j = 0; j < 33; ++j) {
      const char* ap = smem + PB_A + (j & 1) * 16 * AST + l15 * AST + q * 16;
      f32x4 c0 = {0.f, 0.f, 0.f, 0.f}, c1 = c0;
#pragma unroll
      for (int ks = 0; ks < 20; ++ks) {
        bf16x8 a = lds_frag(ap + ks * 64);
        c0 = mfma16(a, wb[ks], c0);
        c1 = mfma16(a, wb[20 + ks], c1);
      }
      char* R = smem + PB_R + (j % 3) * 16 * RST;
#pragma unroll
      for (int r = 0; r < 4; ++r) {
        *(u16*)(R + (4 * q + r) * RST + (32 * w + l15) * 2) = f2bf(c0[r]);
        if (w < 7) *(u16*)(R + (4 * q + r) * RST + (32 * w + 16 + l15) * 2) = f2bf(c1[r]);
      }
      if (j + 1 < 33) { putA((j + 1) & 1); if (j + 2 < 33) fetchA(j + 2); }
      __syncthreads();
      if (j >= 2 && t < 160) {
        const int row = t / 10, ch = t % 10;
        v4u v = *(const v4u*)(smem + PB_O + ((j - 1) & 1) * 16 * OST + row * OST + ch * 16);
        st16_sc1(ws + OFF_OB + (size_t)(512 * rb - 16 + 16 * (j - 1) + row) * 1280 + (80 * g) * 2 + ch * 16, v);
      }
      if (j >= 1) {
        const int item = t >> 3, wi = t & 7, row = item >> 2, hh = item & 3;
        const int srow = 512 * rb - 16 + 16 * j + row;
        const int krow = row - 2 * wi;
        const bool vis = (srow - 2 * wi) >= 0;
        const char* Rc = smem + PB_R + (j % 3) * 16 * RST;
        const char* Rp = smem + PB_R + ((j + 2) % 3) * 16 * RST;
        const char* qrow = Rc + row * RST + (20 * hh) * 2;
        const char* krowp = (krow >= 0 ? Rc + krow * RST : Rp + (krow + 16) * RST);
        float dot = 0.f;
#pragma unroll
        for (int d = 0; d < 20; d += 2) {
          unsigned qv = *(const unsigned*)(qrow + d * 2);
          unsigned kv = *(const unsigned*)(krowp + (80 + 20 * hh + d) * 2);
          dot += bf2f((u16)qv) * bf2f((u16)kv);
          dot += bf2f((u16)(qv >> 16)) * bf2f((u16)(kv >> 16));
        }
        float lg = rbf(rbf(dot) * scale);
        float mx = vis ? lg : -3.0e38f;
        mx = fmaxf(mx, __shfl_xor(mx, 1)); mx = fmaxf(mx, __shfl_xor(mx, 2)); mx = fmaxf(mx, __shfl_xor(mx, 4));
        float e = vis ? __expf(lg - mx) : 0.f;
        float sum = e;
        sum += __shfl_xor(sum, 1); sum += __shfl_xor(sum, 2); sum += __shfl_xor(sum, 4);
        const float pw = rbf(e * (1.0f / sum));
        float o0 = 0.f, o1 = 0.f, o2 = 0.f;
#pragma unroll
        for (int ww = 0; ww < 8; ++ww) {
          const float pv = __shfl(pw, (lane & ~7) + ww);
          const int kr = row - 2 * ww;
          const char* vr = (kr >= 0 ? Rc + kr * RST : Rp + (kr + 16) * RST) + (160 + 20 * hh) * 2;
          o0 += pv * bf2f(*(const u16*)(vr + wi * 2));
          o1 += pv * bf2f(*(const u16*)(vr + (wi + 8) * 2));
          if (wi < 4) o2 += pv * bf2f(*(const u16*)(vr + (wi + 16) * 2));
        }
        char* O = smem + PB_O + (j & 1) * 16 * OST + row * OST + (20 * hh) * 2;
        *(u16*)(O + wi * 2) = f2bf(o0);
        *(u16*)(O + (wi + 8) * 2) = f2bf(o1);
        if (wi < 4) *(u16*)(O + (wi + 16) * 2) = f2bf(o2);
      }
    }
    __syncthreads();
    if (t < 160) {
      const int row = t / 10, ch = t % 10;
      v4u v = *(const v4u*)(smem + PB_O + (32 & 1) * 16 * OST + row * OST + ch * 16);
      st16_sc1(ws + OFF_OB + (size_t)(512 * rb - 16 + 16 * 32 + row) * 1280 + (80 * g) * 2 + ch * 16, v);
    }
    drain();
    __syncthreads();
    if (t == 0) __hip_atomic_fetch_add((unsigned*)(ws + OFF_CNTB + rb * 64), 1u, __ATOMIC_RELAXED, __HIP_MEMORY_SCOPE_AGENT);
  }
  {
    char* stg = smem + 102400 + w * 7168;
    for (int f0 = w * 13; f0 < w * 13 + 13 && f0 < 100; f0 += 7) {
      v4u tmp[7];
#pragma unroll
      for (int i = 0; i < 7; ++i) {
        const int f = f0 + i; const bool okf = (f < w * 13 + 13) && (f < 100);
        const int ff = okf ? f : 0;
        tmp[i] = *(const v4u*)(P.wo + (size_t)((ff % 20) * 32 + (lane >> 1)) * DM + 80 * g + 16 * (ff / 20) + 8 * (lane & 1));
      }
#pragma unroll
      for (int i = 0; i < 7; ++i) *(v4u*)(stg + i * 1024 + lane * 16) = tmp[i];
      __builtin_amdgcn_s_waitcnt(0xC07F);
#pragma unroll
      for (int i = 0; i < 7; ++i) {
        const int f = f0 + i; const bool okf = (f < w * 13 + 13) && (f < 100);
        s16x4 lo = tr_read(stg + i * 1024 + (8 * gg + qq) * 32 + pp * 8);
        s16x4 hi = tr_read(stg + i * 1024 + (8 * gg + 4 + qq) * 32 + pp * 8);
        if (okf) *(bf16x8*)(smem + f * 1024 + lane * 16) = cat4(lo, hi);
      }
      __builtin_amdgcn_s_waitcnt(0xC07F);
    }
    __syncthreads();
    wait_word((const unsigned*)(ws + OFF_CNTB + rb * 64), 7u, dead);
    for (int i = 0; i < 4; ++i) {
      const int row0 = 512 * rb + 16 * (w + 8 * i);
      v4u a0[10], a1[10];
      ld16x10_sc1(ws + OFF_OB + (size_t)(row0 + l15) * 1280 + q * 16, a0);
      ld16x10_sc1(ws + OFF_OB + (size_t)(row0 + l15) * 1280 + 640 + q * 16, a1);
#pragma unroll
      for (int n = 0; n < 5; ++n) {
        f32x4 acc = {0.f, 0.f, 0.f, 0.f};
#pragma unroll
        for (int ks = 0; ks < 10; ++ks) acc = mfma16(as_frag(a0[ks]), lds_frag(smem + (n * 20 + ks) * 1024 + lane * 16), acc);
#pragma unroll
        for (int ks = 0; ks < 10; ++ks) acc = mfma16(as_frag(a1[ks]), lds_frag(smem + (n * 20 + 10 + ks) * 1024 + lane * 16), acc);
#pragma unroll
        for (int r = 0; r < 4; ++r) P.out[(size_t)(row0 + 4 * q + r) * OUTW + DM + 80 * g + 16 * n + l15] = f2bf(acc[r]);
      }
    }
  }
}

__global__ __launch_bounds__(512, 2) void fused_kernel(Params P) {
  extern __shared__ __attribute__((aligned(16))) char smem[];
  const int b = blockIdx.x;
  STAMP(63, 7);
  if (b < 32) role_s1(P, smem, b);
  else if (b < 72) role_s2(P, smem, b - 32);
  else if (b < 136) role_s4(P, smem, (b - 72) & 31, (b - 72) >> 5);
  else if (b < 216) role_s3(P, smem, (b - 136) % 40, (b - 136) / 40);
  else if (b < 248) role_pb(P, smem, (b - 216) & 7, (b - 216) >> 3);
  STAMP(63, 6);
}

extern "C" void kernel_launch(void* const* d_in, const int* in_sizes, int n_in,
                              void* d_out, int out_size, void* d_ws, size_t ws_size,
                              hipStream_t stream) {
  Params P;
  P.ax = (const u16*)d_in[0]; P.mem0 = (const u16*)d_in[1];
  P.Wq = (const u16*)d_in[2]; P.Wk = (const u16*)d_in[3]; P.Wv = (const u16*)d_in[4]; P.Wo = (const u16*)d_in[5];
  P.bx = (const u16*)d_in[7]; P.wqkv = (const u16*)d_in[8]; P.wo = (const u16*)d_in[9];
  P.out = (u16*)d_out; P.ws = (char*)d_ws;
  hipFuncSetAttribute((const void*)fused_kernel, hipFuncAttributeMaxDynamicSharedMemorySize, LDS_BYTES);
  hipMemsetAsync(d_ws, 0xFF, POISON_BYTES, stream);
  fused_kernel<<<256, 512, LDS_BYTES, stream>>>(P);
}
```

```cpp
#include <hip/hip_runtime.h>
#include <hip/hip_bf16.h>
#include <stdint.h>

typedef unsigned short u16;
typedef short bf16x8 __attribute__((ext_vector_type(8)));
typedef short s16x4 __attribute__((ext_vector_type(4)));
typedef float f32x4 __attribute__((ext_vector_type(4)));
typedef unsigned v4u __attribute__((ext_vector_type(4)));
typedef unsigned v2u __attribute__((ext_vector_type(2)));
#define LAS __attribute__((address_space(3)))
#define DI __device__ __forceinline__

constexpr int SLEN = 2048, DM = 640, NH = 32, DH = 128, HD = 4096, NSEG = 64;
constexpr int OUTW = 1280;
constexpr size_t OFF_FLAGKV = 0;
constexpr size_t OFF_FLAGM  = 8192;
constexpr size_t OFF_CNTOX  = 16384;
constexpr size_t OFF_CNTB   = 20480;
constexpr size_t OFF_MEMBUF = 24576;
constexpr size_t OFF_OBUF   = OFF_MEMBUF + 64 * 10240;
constexpr size_t POISON_BYTES = OFF_OBUF + (size_t)64 * 65536;
constexpr size_t OFF_KX  = (size_t)8 << 20;
constexpr size_t OFF_VXT = (size_t)24 << 20;
constexpr size_t OFF_QX  = (size_t)40 << 20;
constexpr size_t OFF_KM  = (size_t)56 << 20;
constexpr size_t OFF_VMT = (size_t)60 << 20;
constexpr size_t OFF_OX  = (size_t)64 << 20;
constexpr size_t OFF_OB  = (size_t)80 << 20;
constexpr unsigned SENT = 0xFFFFFFFFu;
constexpr int SPIN_CAP = 300000;
constexpr int LDS_BYTES = 160 * 1024;
#define POLL_SLEEP 60
#define S1_PRE 56
#define S2_PRE 106
#define S3_SLEEP 20

#define STAMP(s, i) do {} while (0)
DI float bf2f(u16 b) { return __builtin_bit_cast(float, (unsigned)b << 16); }
DI u16 f2bf(float f) { __bf16 b = (__bf16)f; return __builtin_bit_cast(u16, b); }
DI float rbf(float f) { return bf2f(f2bf(f)); }
DI unsigned pack2(float a, float b) { return (unsigned)f2bf(a) | ((unsigned)f2bf(b) << 16); }
DI unsigned unsent(unsigned x) { return x == SENT ? 0xFFFEFFFFu : x; }
DI bool valid4(v4u v) { return v.x != SENT && v.y != SENT && v.z != SENT && v.w != SENT; }
DI f32x4 mfma16(bf16x8 a, bf16x8 b, f32x4 c) { return __builtin_amdgcn_mfma_f32_16x16x32_bf16(a, b, c, 0, 0, 0); }
DI bf16x8 as_frag(v4u v) { return __builtin_bit_cast(bf16x8, v); }
DI s16x4 tr_read(const char* p) {
  typedef short v4i16_t __attribute__((ext_vector_type(4)));
  return __builtin_amdgcn_ds_read_tr16_b64_v4i16((LAS v4i16_t*)(p));
}
DI float red_max(float x) {
  unsigned u = __builtin_bit_cast(unsigned, x);
  auto r = __builtin_amdgcn_permlane16_swap(u, u, false, false);
  x = fmaxf(__builtin_bit_cast(float, (unsigned)r[0]), __builtin_bit_cast(float, (unsigned)r[1]));
  u = __builtin_bit_cast(unsigned, x);
  auto r2 = __builtin_amdgcn_permlane32_swap(u, u, false, false);
  return fmaxf(__builtin_bit_cast(float, (unsigned)r2[0]), __builtin_bit_cast(float, (unsigned)r2[1]));
}
DI float red_sum(float x) {
  unsigned u = __builtin_bit_cast(unsigned, x);
  auto r = __builtin_amdgcn_permlane16_swap(u, u, false, false);
  x = __builtin_bit_cast(float, (unsigned)r[0]) + __builtin_bit_cast(float, (unsigned)r[1]);
  u = __builtin_bit_cast(unsigned, x);
  auto r2 = __builtin_amdgcn_permlane32_swap(u, u, false, false);
  return __builtin_bit_cast(float, (unsigned)r2[0]) + __builtin_bit_cast(float, (unsigned)r2[1]);
}
DI bf16x8 cat4(s16x4 lo, s16x4 hi) { bf16x8 r; r[0]=lo[0]; r[1]=lo[1]; r[2]=lo[2]; r[3]=lo[3]; r[4]=hi[0]; r[5]=hi[1]; r[6]=hi[2]; r[7]=hi[3]; return r; }
DI bf16x8 lds_frag(const char* p) { return *(const bf16x8*)p; }

DI v4u ld16_sc1(const void* p) { v4u r; asm volatile("global_load_dwordx4 %0, %1, off sc1\n\ts_waitcnt vmcnt(0)" : "=&v"(r) : "v"(p) : "memory"); return r; }
DI void ld16x2_sc1(const void* p0, const void* p1, v4u& a, v4u& b) {
  asm volatile("global_load_dwordx4 %0, %2, off sc1\n\tglobal_load_dwordx4 %1, %3, off sc1\n\ts_waitcnt vmcnt(0)"
               : "=&v"(a), "=&v"(b) : "v"(p0), "v"(p1) : "memory");
}
DI unsigned ld4_sc1(const void* p) { unsigned r; asm volatile("global_load_dword %0, %1, off sc1\n\ts_waitcnt vmcnt(0)" : "=&v"(r) : "v"(p) : "memory"); return r; }
DI void st16_sc1(void* p, v4u v) { asm volatile("global_store_dwordx4 %0, %1, off sc1\n\ts_nop 1" :: "v"(p), "v"(v) : "memory"); }
DI void st8_sc1(void* p, v2u v) { asm volatile("global_store_dwordx2 %0, %1, off sc1\n\ts_nop 1" :: "v"(p), "v"(v) : "memory"); }
DI void st4_sc1(void* p, unsigned v) { asm volatile("global_store_dword %0, %1, off sc1\n\ts_nop 1" :: "v"(p), "v"(v) : "memory"); }
DI float dpp_xor1(float x) { return __builtin_bit_cast(float, __builtin_amdgcn_update_dpp(0, __builtin_bit_cast(int, x), 0xB1, 0xf, 0xf, true)); }
DI float dpp_xor2(float x) { return __builtin_bit_cast(float, __builtin_amdgcn_update_dpp(0, __builtin_bit_cast(int, x), 0x4E, 0xf, 0xf, true)); }
DI float dpp_shl4(float x) { return __builtin_bit_cast(float, __builtin_amdgcn_update_dpp(0, __builtin_bit_cast(int, x), 0x104, 0xf, 0xf, true)); }
template <int CTRL> DI float dpp_ror(float x) { return __builtin_bit_cast(float, __builtin_amdgcn_update_dpp(0, __builtin_bit_cast(int, x), CTRL, 0xf, 0xf, true)); }
DI float row_sum16(float x) { x += dpp_ror<0x128>(x); x += dpp_ror<0x124>(x); x += dpp_ror<0x122>(x); x += dpp_ror<0x121>(x); return x; }
DI void drain() { asm volatile("s_waitcnt vmcnt(0)" ::: "memory"); }
DI void ld16x8s1k_sc1(const char* p, v4u (&a)[8]) {
  asm volatile(
    "global_load_dwordx4 %0, %8, off sc1\n\t"
    "global_load_dwordx4 %1, %8, off offset:1024 sc1\n\t"
    "global_load_dwordx4 %2, %8, off offset:2048 sc1\n\t"
    "global_load_dwordx4 %3, %8, off offset:3072 sc1\n\t"
    "global_load_dwordx4 %4, %9, off sc1\n\t"
    "global_load_dwordx4 %5, %9, off offset:1024 sc1\n\t"
    "global_load_dwordx4 %6, %9, off offset:2048 sc1\n\t"
    "global_load_dwordx4 %7, %9, off offset:3072 sc1\n\t"
    "s_waitcnt vmcnt(0)"
    : "=&v"(a[0]), "=&v"(a[1]), "=&v"(a[2]), "=&v"(a[3]), "=&v"(a[4]), "=&v"(a[5]), "=&v"(a[6]), "=&v"(a[7])
    : "v"(p), "v"(p + 4096) : "memory");
}
DI void ld16x8s512_sc1(const char* p, v4u (&a)[8]) {
  asm volatile(
    "global_load_dwordx4 %0, %8, off sc1\n\t"
    "global_load_dwordx4 %1, %8, off offset:512 sc1\n\t"
    "global_load_dwordx4 %2, %8, off offset:1024 sc1\n\t"
    "global_load_dwordx4 %3, %8, off offset:1536 sc1\n\t"
    "global_load_dwordx4 %4, %8, off offset:2048 sc1\n\t"
    "global_load_dwordx4 %5, %8, off offset:2560 sc1\n\t"
    "global_load_dwordx4 %6, %8, off offset:3072 sc1\n\t"
    "global_load_dwordx4 %7, %8, off offset:3584 sc1\n\t"
    "s_waitcnt vmcnt(0)"
    : "=&v"(a[0]), "=&v"(a[1]), "=&v"(a[2]), "=&v"(a[3]), "=&v"(a[4]), "=&v"(a[5]), "=&v"(a[6]), "=&v"(a[7])
    : "v"(p) : "memory");
}
DI void ld16x10_sc1(const char* p, v4u (&a)[10]) {
  asm volatile(
    "global_load_dwordx4 %0, %10, off sc1\n\t"
    "global_load_dwordx4 %1, %10, off offset:64 sc1\n\t"
    "global_load_dwordx4 %2, %10, off offset:128 sc1\n\t"
    "global_load_dwordx4 %3, %10, off offset:192 sc1\n\t"
    "global_load_dwordx4 %4, %10, off offset:256 sc1\n\t"
    "global_load_dwordx4 %5, %10, off offset:320 sc1\n\t"
    "global_load_dwordx4 %6, %10, off offset:384 sc1\n\t"
    "global_load_dwordx4 %7, %10, off offset:448 sc1\n\t"
    "global_load_dwordx4 %8, %10, off offset:512 sc1\n\t"
    "global_load_dwordx4 %9, %10, off offset:576 sc1\n\t"
    "s_waitcnt vmcnt(0)"
    : "=&v"(a[0]), "=&v"(a[1]), "=&v"(a[2]), "=&v"(a[3]), "=&v"(a[4]), "=&v"(a[5]), "=&v"(a[6]), "=&v"(a[7]),
      "=&v"(a[8]), "=&v"(a[9])
    : "v"(p) : "memory");
}
DI void wait_word(const unsigned* p, unsigned want, int& dead, bool fast = false) {
  if ((threadIdx.x >> 6) == 0 && !dead) {
    int sp = 0;
    for (; sp < SPIN_CAP; ++sp) {
      unsigned v = ld4_sc1(p);
      if (__builtin_amdgcn_readfirstlane(v) == want) break;
      if (fast) __builtin_amdgcn_s_sleep(4); else __builtin_amdgcn_s_sleep(POLL_SLEEP);
    }
    if (sp >= SPIN_CAP) dead = 1;
  }
  __syncthreads();
}

constexpr int AST = 1296;
constexpr int KST = 272;
constexpr int VST = 136;
constexpr int L_WRES = 0;
constexpr int L_STG = 98304;
constexpr int STG_BUF = 3 * 32 * KST;
constexpr int S1_A = 98304;
constexpr int S1_K = S1_A + 10496;
constexpr int S1_VT = S1_K + 48 * KST;
constexpr int S1_Q = S1_VT + 128 * VST;
constexpr int S4_A = 98304;
constexpr int S4_T = S4_A + 2 * 16 * AST;
constexpr int TST = 528;

DI void load_w3(const u16* __restrict__ Wq, const u16* __restrict__ Wk, const u16* __restrict__ Wv, int h,
                char* smem, bf16x8 (&wr)[48], int t) {
  const int lane = t & 63, w = t >> 6;
  const int r = t >> 4, c16 = t & 15;
  const size_t goff = (size_t)r * HD + h * DH + c16 * 8;
  const int g = lane >> 4, qq = (lane & 15) >> 2, p = lane & 3;
  const int troff = (8 * g + qq) * KST + (16 * w + 4 * p) * 2;
  v4u pf[4][3];
#pragma unroll
  for (int i = 0; i < 4; ++i) {
    pf[i][0] = *(const v4u*)(Wq + goff + (size_t)i * 32 * HD);
    pf[i][1] = *(const v4u*)(Wk + goff + (size_t)i * 32 * HD);
    pf[i][2] = *(const v4u*)(Wv + goff + (size_t)i * 32 * HD);
  }
#pragma unroll
  for (int ks = 0; ks < 20; ++ks) {
    char* stg = smem + L_STG + (ks & 1) * STG_BUF;
#pragma unroll
    for (int m = 0; m < 3; ++m) *(v4u*)(stg + m * 32 * KST + r * KST + c16 * 16) = pf[ks & 3][m];
    if (ks + 4 < 20) {
      pf[ks & 3][0] = *(const v4u*)(Wq + goff + (size_t)(ks + 4) * 32 * HD);
      pf[ks & 3][1] = *(const v4u*)(Wk + goff + (size_t)(ks + 4) * 32 * HD);
      pf[ks & 3][2] = *(const v4u*)(Wv + goff + (size_t)(ks + 4) * 32 * HD);
    }
    __syncthreads();
#pragma unroll
    for (int m = 0; m < 3; ++m) {
      s16x4 lo = tr_read(stg + m * 32 * KST + troff);
      s16x4 hi = tr_read(stg + m * 32 * KST + troff + 4 * KST);
      bf16x8 f = cat4(lo, hi);
      if (ks < 16) wr[ks * 3 + m] = f;
      else *(bf16x8*)(smem + L_WRES + w * 12288 + ((ks - 16) * 3 + m) * 1024 + lane * 16) = f;
    }
  }
  __syncthreads();
}

DI void proj3(const char* ldsA, int arow, const char* smem, const bf16x8 (&wr)[48], int t, f32x4& aq, f32x4& ak, f32x4& av) {
  const int lane = t & 63, w = t >> 6, q = lane >> 4;
  const char* ap = ldsA + arow * AST + q * 16;
  int woff = w * 12288 + lane * 16;
  asm volatile("" : "+v"(woff));
  const char* wp = smem + L_WRES + woff;
  aq = (f32x4){0.f, 0.f, 0.f, 0.f}; ak = aq; av = aq;
#pragma unroll
  for (int ks = 0; ks < 20; ++ks) {
    bf16x8 a = lds_frag(ap + ks * 64);
    bf16x8 b0, b1, b2;
    if (ks < 16) { b0 = wr[ks * 3]; b1 = wr[ks * 3 + 1]; b2 = wr[ks * 3 + 2]; }
    else { b0 = lds_frag(wp + ((ks - 16) * 3) * 1024); b1 = lds_frag(wp + ((ks - 16) * 3 + 1) * 1024); b2 = lds_frag(wp + ((ks - 16) * 3 + 2) * 1024); }
    aq = mfma16(a, b0, aq);
    ak = mfma16(a, b1, ak);
    av = mfma16(a, b2, av);
    if ((ks & 3) == 3) { asm volatile("" : "+v"(aq), "+v"(ak), "+v"(av)); __builtin_amdgcn_sched_barrier(0); }
  }
}

template <class VIS>
DI void softmax_pt(const f32x4 (&st)[3], float scale, VIS vis, bf16x8& pb0, bf16x8& pb1) {
  float s[3][4]; float mx = -3.0e38f;
#pragma unroll
  for (int kt = 0; kt < 3; ++kt)
#pragma unroll
    for (int r = 0; r < 4; ++r) {
      float v = rbf(rbf(st[kt][r]) * scale);
      s[kt][r] = v;
      if (vis(kt, r)) mx = fmaxf(mx, v);
    }
  mx = red_max(mx);
  float sum = 0.f;
#pragma unroll
  for (int kt = 0; kt < 3; ++kt)
#pragma unroll
    for (int r = 0; r < 4; ++r) {
      float e = vis(kt, r) ? __expf(s[kt][r] - mx) : 0.f;
      s[kt][r] = e; sum += e;
    }
  sum = red_sum(sum);
  const float inv = 1.0f / sum;
#pragma unroll
  for (int r = 0; r < 4; ++r) {
    pb0[r] = (short)f2bf(s[0][r] * inv);
    pb0[4 + r] = (short)f2bf(s[1][r] * inv);
    pb1[r] = (short)f2bf(s[2][r] * inv);
    pb1[4 + r] = 0;
  }
}

struct Params {
  const u16* ax; const u16* mem0; const u16* Wq; const u16* Wk; const u16* Wv; const u16* Wo;
  const u16* bx; const u16* wqkv; const u16* wo;
  u16* out; char* ws;
};

DI void role_s1(const Params& P, char* smem, int h) {
  int t = threadIdx.x;
  bf16x8 wr[48];
  load_w3(P.Wq, P.Wk, P.Wv, h, smem, wr, t);
  char* ws = P.ws;
  int dead = 0;
  for (int i = t; i < 8 * 16; i += 512) *(v4u*)(smem + S1_K + (40 + (i >> 4)) * KST + (i & 15) * 16) = (v4u){0, 0, 0, 0};
  for (int i = t; i < 128 * 6; i += 512) *(v2u*)(smem + S1_VT + (i / 6) * VST + 80 + (i % 6) * 8) = (v2u){0, 0};
  const float scale = 0.08838834764831845f;
  for (int s = 0; s < NSEG; ++s) {
    asm volatile("" : "+v"(t));
    const int lane = t & 63, w = t >> 6, q = lane >> 4, l15 = lane & 15;
    {
      wait_word((const unsigned*)(ws + OFF_FLAGKV) + h * 64 + s, 1u, dead);
      const char* kx = ws + OFF_KX + ((size_t)(s * NH + h)) * 8192;
      const char* vx = ws + OFF_VXT + ((size_t)(s * NH + h)) * 8192;
      v4u a, b;
      ld16x2_sc1(kx + t * 16, vx + t * 16, a, b);
      *(v4u*)(smem + S1_K + (8 + (t >> 4)) * KST + (t & 15) * 16) = a;
      v2u b0 = {b.x, b.y}, b1 = {b.z, b.w};
      *(v2u*)(smem + S1_VT + (t >> 2) * VST + 16 + (t & 3) * 16) = b0;
      *(v2u*)(smem + S1_VT + (t >> 2) * VST + 16 + (t & 3) * 16 + 8) = b1;
    }
    STAMP(s, 0);
    {
      const int c0 = t, c1 = 512 + t;
      v4u a, b = {0, 0, 0, 0};
      if (s == 0) {
        a = *(const v4u*)((const char*)P.mem0 + c0 * 16);
        if (w < 2) b = *(const v4u*)((const char*)P.mem0 + c1 * 16);
      } else {
        const char* src = ws + OFF_MEMBUF + (size_t)s * 10240;
        if (S1_PRE > 0) { __builtin_amdgcn_s_sleep(S1_PRE / 2); __builtin_amdgcn_s_sleep(S1_PRE - S1_PRE / 2); }
        for (int sp = 0;; ++sp) {
          bool ok;
          if (w < 2) { ld16x2_sc1(src + c0 * 16, src + c1 * 16, a, b); ok = valid4(a) && valid4(b); }
          else { a = ld16_sc1(src + c0 * 16); ok = valid4(a); }
          if (__all(ok)) break;
          if (dead || sp > SPIN_CAP) { dead = 1; break; }
        }
      }
      if (s == 0) {
        *(v4u*)(smem + S1_A + (c0 / 80) * AST + (c0 % 80) * 16) = a;
        if (w < 2) *(v4u*)(smem + S1_A + (c1 / 80) * AST + (c1 % 80) * 16) = b;
      } else {
        *(v4u*)(smem + S1_A + ((c0 >> 1) & 7) * AST + (c0 >> 4) * 32 + (c0 & 1) * 16) = a;
        if (w < 2) *(v4u*)(smem + S1_A + ((c1 >> 1) & 7) * AST + (c1 >> 4) * 32 + (c1 & 1) * 16) = b;
      }
    }
    __syncthreads();
    STAMP(s, 1);
    f32x4 aq, ak, av;
    proj3(smem + S1_A, lane & 7, smem, wr, t, aq, ak, av);
    if (q < 2) {
#pragma unroll
      for (int r = 0; r < 4; ++r) {
        *(u16*)(smem + S1_Q + (4 * q + r) * KST + (16 * w + l15) * 2) = f2bf(aq[r]);
        *(u16*)(smem + S1_K + (4 * q + r) * KST + (16 * w + l15) * 2) = f2bf(ak[r]);
      }
      v2u vv = {pack2(av[0], av[1]), pack2(av[2], av[3])};
      *(v2u*)(smem + S1_VT + (16 * w + l15) * VST + q * 8) = vv;
    }
    __syncthreads();
    STAMP(s, 2);
    if (s < NSEG - 1 && w < 4) {
      const char* qp = smem + S1_Q + (lane & 7) * KST + q * 16;
      const char* kp = smem + S1_K + l15 * KST + q * 16;
      f32x4 st[3];
      st[0] = (f32x4){0.f, 0.f, 0.f, 0.f}; st[1] = st[0]; st[2] = st[0];
      bf16x8 qf0 = lds_frag(qp), ka0 = lds_frag(kp), kb0 = lds_frag(kp + 16 * KST), kc0 = lds_frag(kp + 32 * KST);
      bf16x8 qf1 = lds_frag(qp + 64), ka1 = lds_frag(kp + 64), kb1 = lds_frag(kp + 16 * KST + 64), kc1 = lds_frag(kp + 32 * KST + 64);
      st[0] = mfma16(ka0, qf0, st[0]); st[1] = mfma16(kb0, qf0, st[1]); st[2] = mfma16(kc0, qf0, st[2]);
      qf0 = lds_frag(qp + 128); ka0 = lds_frag(kp + 128); kb0 = lds_frag(kp + 16 * KST + 128); kc0 = lds_frag(kp + 32 * KST + 128);
      st[0] = mfma16(ka1, qf1, st[0]); st[1] = mfma16(kb1, qf1, st[1]); st[2] = mfma16(kc1, qf1, st[2]);
      qf1 = lds_frag(qp + 192); ka1 = lds_frag(kp + 192); kb1 = lds_frag(kp + 16 * KST + 192); kc1 = lds_frag(kp + 32 * KST + 192);
      st[0] = mfma16(ka0, qf0, st[0]); st[1] = mfma16(kb0, qf0, st[1]); st[2] = mfma16(kc0, qf0, st[2]);
      const char* vp = smem + S1_VT + (32 * w + l15) * VST + q * 8;
      s16x4 v0 = *(const s16x4*)(vp), v1 = *(const s16x4*)(vp + 32), v2 = *(const s16x4*)(vp + 64);
      s16x4 u0 = *(const s16x4*)(vp + 16 * VST), u1 = *(const s16x4*)(vp + 16 * VST + 32), u2 = *(const s16x4*)(vp + 16 * VST + 64);
      st[0] = mfma16(ka1, qf1, st[0]); st[1] = mfma16(kb1, qf1, st[1]); st[2] = mfma16(kc1, qf1, st[2]);
      bf16x8 pb0, pb1;
      softmax_pt(st, scale, [&](int kt, int r) { return kt < 2 || q < 2; }, pb0, pb1);
      const s16x4 z4 = {0, 0, 0, 0};
      f32x4 o = {0.f, 0.f, 0.f, 0.f}, o2 = o;
      o = mfma16(cat4(v0, v1), pb0, o);
      o2 = mfma16(cat4(u0, u1), pb0, o2);
      o = mfma16(cat4(v2, z4), pb1, o);
      o2 = mfma16(cat4(u2, z4), pb1, o2);
      {
        unsigned o00 = unsent(pack2(o[0], o[1])), o01 = unsent(pack2(o[2], o[3]));
        unsigned o10 = unsent(pack2(o2[0], o2[1])), o11 = unsent(pack2(o2[2], o2[3]));
        auto r0 = __builtin_amdgcn_permlane16_swap(o00, o00, false, false);
        auto r1 = __builtin_amdgcn_permlane16_swap(o01, o01, false, false);
        auto r2 = __builtin_amdgcn_permlane16_swap(o10, o10, false, false);
        auto r3 = __builtin_amdgcn_permlane16_swap(o11, o11, false, false);
        if (l15 < 8) {
          char* dst = ws + OFF_OBUF + (size_t)s * 65536 + h * 2048 + (2 * w + (q & 1)) * 256 + l15 * 32 + (q >> 1) * 16;
          v4u ov = (q & 1) ? (v4u){(unsigned)r2[0], (unsigned)r3[0], (unsigned)r2[1], (unsigned)r3[1]}
                           : (v4u){(unsigned)r0[0], (unsigned)r1[0], (unsigned)r0[1], (unsigned)r1[1]};
          st16_sc1(dst, ov);
        }
      }
    }
    STAMP(s, 3);
    if (t < 128) {
      v4u kv = *(const v4u*)(smem + S1_K + (t >> 4) * KST + (t & 15) * 16);
      st16_sc1(ws + OFF_KM + ((size_t)(s * NH + h)) * 2048 + t * 16, kv);
    } else if (t < 256) {
      const int d = t - 128;
      v2u a = *(const v2u*)(smem + S1_VT + d * VST), b = *(const v2u*)(smem + S1_VT + d * VST + 8);
      v4u vv = {a.x, a.y, b.x, b.y};
      st16_sc1(ws + OFF_VMT + ((size_t)(s * NH + h)) * 2048 + d * 16, vv);
    }
    drain();
    __syncthreads();
    if (t == 0) st4_sc1((unsigned*)(ws + OFF_FLAGM) + h * 64 + s, 1u);
    STAMP(s, 4);
  }
}

DI void load_wo16(const u16* __restrict__ Wo, int c, char* smem, bf16x8 (&wb)[16], int t) {
  const int lane = t & 63, w = t >> 6;
  const u16* src = Wo + (size_t)(512 * w + (lane >> 1)) * DM + 16 * c + 8 * (lane & 1);
  v4u tmp[16];
#pragma unroll
  for (int ks = 0; ks < 16; ++ks) tmp[ks] = *(const v4u*)(src + (size_t)ks * 32 * DM);
  char* base = smem + w * 16384;
#pragma unroll
  for (int ks = 0; ks < 16; ++ks) *(v4u*)(base + ks * 1024 + lane * 16) = tmp[ks];
  __syncthreads();
  const int g = lane >> 4, qq = (lane & 15) >> 2, p = lane & 3;
#pragma unroll
  for (int ks = 0; ks < 16; ++ks) {
    s16x4 lo = tr_read(base + ks * 1024 + (8 * g + qq) * 32 + p * 8);
    s16x4 hi = tr_read(base + ks * 1024 + (8 * g + 4 + qq) * 32 + p * 8);
    wb[ks] = cat4(lo, hi);
  }
  __syncthreads();
}

DI void role_s2(const Params& P, char* smem, int c) {
  const int t = threadIdx.x, lane = t & 63, w = t >> 6, q = lane >> 4, l15 = lane & 15;
  bf16x8 wb[16];
  load_wo16(P.Wo, c, smem, wb, t);
  char* ws = P.ws;
  int dead = 0;
  float* red = (float*)smem;
  for (int s = 0; s < NSEG - 1; ++s) {
    v4u a[8];
    const char* src = ws + OFF_OBUF + (size_t)s * 65536 + w * 8192 + (l15 >> 3) * 4096 + (q >> 1) * 256 + (l15 & 7) * 32 + (q & 1) * 16;
    const char* prb = ws + OFF_OBUF + (size_t)s * 65536 + w * 8192 + (lane & 31) * 256 + 252;
    if (S2_PRE > 0 && s > 0) { __builtin_amdgcn_s_sleep(S2_PRE / 2); __builtin_amdgcn_s_sleep(S2_PRE - S2_PRE / 2); }
    for (int sp = 0;; ++sp) {
      unsigned pv = ld4_sc1(prb);
      if (__any(pv != SENT)) break;
      if (dead || sp > SPIN_CAP) { dead = 1; break; }
    }
    {
      bool ok = false;
      for (int sp = 0;; ++sp) {
        if (!ok) {
          ld16x8s512_sc1(src, a);
          ok = true;
#pragma unroll
          for (int i = 0; i < 8; ++i) ok = ok && valid4(a[i]);
        }
        if (__all(ok)) break;
        if (dead || sp > SPIN_CAP) { dead = 1; break; }
      }
    }
    STAMP(s, 0);
    f32x4 acc0 = {0.f, 0.f, 0.f, 0.f}, acc1 = acc0;
#pragma unroll
    for (int i = 0; i < 8; ++i) {
      acc0 = mfma16(as_frag(a[i]), wb[i], acc0);
      acc1 = mfma16(as_frag(a[i]), wb[i + 8], acc1);
    }
    float* rb = red + (s & 1) * 2048 + w * 256 + (q >> 1) * 128;
#pragma unroll
    for (int r = 0; r < 4; ++r) rb[(4 * (q & 1) + r) * 16 + l15] = (q < 2) ? acc0[r] : acc1[r];
    __syncthreads();
    STAMP(s, 1);
    if (t < 256) {
      const int g = t >> 4, pi = t & 15;
      const float* rp = red + (s & 1) * 2048 + pi * 128 + g * 8;
      const f32x4 x0 = *(const f32x4*)rp, x1 = *(const f32x4*)(rp + 4);
      float y[8];
#pragma unroll
      for (int i = 0; i < 4; ++i) { y[i] = row_sum16(x0[i]); y[4 + i] = row_sum16(x1[i]); }
      if (pi == 0) {
        v4u o = {unsent(pack2(y[0], y[1])), unsent(pack2(y[2], y[3])), unsent(pack2(y[4], y[5])), unsent(pack2(y[6], y[7]))};
        st16_sc1(ws + OFF_MEMBUF + (size_t)(s + 1) * 10240 + c * 256 + g * 16, o);
      }
    }
    STAMP(s, 2);
  }
}

DI void role_s4(const Params& P, char* smem, int h, int par) {
  const int t = threadIdx.x, lane = t & 63, w = t >> 6, q = lane >> 4, l15 = lane & 15;
  char* ws = P.ws;
  int dead = 0;
  {
    bf16x8 wr[48];
    load_w3(P.Wq, P.Wk, P.Wv, h, smem, wr, t);
    v4u pa[3];
    auto fetchA = [&](int j) {
      const int row0 = (2 * (j >> 1) + par) * 32 + (j & 1) * 16;
      const char* src = (const char*)P.ax + (size_t)row0 * (DM * 2);
      pa[0] = *(const v4u*)(src + t * 16);
      pa[1] = *(const v4u*)(src + (512 + t) * 16);
      if (t < 256) pa[2] = *(const v4u*)(src + (1024 + t) * 16);
    };
    auto putA = [&](int buf) {
      char* dst = smem + S4_A + buf * 16 * AST;
      *(v4u*)(dst + (t / 80) * AST + (t % 80) * 16) = pa[0];
      *(v4u*)(dst + ((512 + t) / 80) * AST + ((512 + t) % 80) * 16) = pa[1];
      if (t < 256) *(v4u*)(dst + ((1024 + t) / 80) * AST + ((1024 + t) % 80) * 16) = pa[2];
    };
    fetchA(0); putA(0); fetchA(1);
    __syncthreads();
    for (int j = 0; j < 64; ++j) {
      const int seg = 2 * (j >> 1) + par, mt = j & 1;
      f32x4 aq, ak, av;
      proj3(smem + S4_A + (j & 1) * 16 * AST, l15, smem, wr, t, aq, ak, av);
      char* T = smem + S4_T + (j & 1) * 16 * TST;
#pragma unroll
      for (int r = 0; r < 4; ++r) {
        *(u16*)(T + (4 * q + r) * TST + (16 * w + l15) * 2) = f2bf(aq[r]);
        *(u16*)(T + (4 * q + r) * TST + 256 + (16 * w + l15) * 2) = f2bf(ak[r]);
      }
      {
        v2u vv = {pack2(av[0], av[1]), pack2(av[2], av[3])};
        st8_sc1(ws + OFF_VXT + ((size_t)(seg * NH + h)) * 8192 + (16 * w + l15) * 64 + (16 * mt + 4 * q) * 2, vv);
      }
      if (j + 1 < 64) { putA((j + 1) & 1); if (j + 2 < 64) fetchA(j + 2); }
      __syncthreads();
      {
        const int row = t >> 5, ch = t & 31;
        v4u v = *(const v4u*)(T + row * TST + ch * 16);
        const size_t o = ((size_t)(seg * NH + h)) * 8192 + (16 * mt + row) * 256 + (ch & 15) * 16;
        if (ch < 16) *(v4u*)(ws + OFF_QX + o) = v; else st16_sc1(ws + OFF_KX + o, v);
      }
      if (mt == 1) {
        drain();
        __syncthreads();
        if (t == 0) st4_sc1((unsigned*)(ws + OFF_FLAGKV) + h * 64 + seg, 1u);
        STAMP(j >> 1, 0);
      }
    }
  }
  const float scale = 0.08838834764831845f;
  const int qt = w & 1, dq = w >> 1;
  for (int i = 0; i < 32; ++i) {
    const int seg = 2 * i + par;
    wait_word((const unsigned*)(ws + OFF_FLAGM) + h * 64 + seg, 1u, dead, seg == NSEG - 1);
    const size_t sh = (size_t)(seg * NH + h);
    const char* km = ws + OFF_KM + sh * 2048;
    const char* vm = ws + OFF_VMT + sh * 2048;
    const char* kx = ws + OFF_KX + sh * 8192;
    const char* vx = ws + OFF_VXT + sh * 8192;
    const char* qx = ws + OFF_QX + sh * 8192;
    v4u kf[3][4], qf[4];
    {
      const char* p0 = (l15 < 8) ? (km + l15 * 256) : (kx + (l15 - 8) * 256);
      const char* p1 = kx + (8 + l15) * 256;
      const char* p2 = kx + (24 + (l15 & 7)) * 256;
      const char* pq = qx + (16 * qt + l15) * 256;
      v4u tmp[16];
      asm volatile(
        "global_load_dwordx4 %0, %16, off sc1\n\t"
        "global_load_dwordx4 %1, %16, off offset:64 sc1\n\t"
        "global_load_dwordx4 %2, %16, off offset:128 sc1\n\t"
        "global_load_dwordx4 %3, %16, off offset:192 sc1\n\t"
        "global_load_dwordx4 %4, %17, off sc1\n\t"
        "global_load_dwordx4 %5, %17, off offset:64 sc1\n\t"
        "global_load_dwordx4 %6, %17, off offset:128 sc1\n\t"
        "global_load_dwordx4 %7, %17, off offset:192 sc1\n\t"
        "global_load_dwordx4 %8, %18, off sc1\n\t"
        "global_load_dwordx4 %9, %18, off offset:64 sc1\n\t"
        "global_load_dwordx4 %10, %18, off offset:128 sc1\n\t"
        "global_load_dwordx4 %11, %18, off offset:192 sc1\n\t"
        "global_load_dwordx4 %12, %19, off sc1\n\t"
        "global_load_dwordx4 %13, %19, off offset:64 sc1\n\t"
        "global_load_dwordx4 %14, %19, off offset:128 sc1\n\t"
        "global_load_dwordx4 %15, %19, off offset:192 sc1\n\t"
        "s_waitcnt vmcnt(0)"
        : "=&v"(tmp[0]), "=&v"(tmp[1]), "=&v"(tmp[2]), "=&v"(tmp[3]), "=&v"(tmp[4]), "=&v"(tmp[5]), "=&v"(tmp[6]), "=&v"(tmp[7]),
          "=&v"(tmp[8]), "=&v"(tmp[9]), "=&v"(tmp[10]), "=&v"(tmp[11]), "=&v"(tmp[12]), "=&v"(tmp[13]), "=&v"(tmp[14]), "=&v"(tmp[15])
        : "v"(p0 + q * 16), "v"(p1 + q * 16), "v"(p2 + q * 16), "v"(pq + q * 16) : "memory");
#pragma unroll
      for (int ks = 0; ks < 4; ++ks) { kf[0][ks] = tmp[ks]; kf[1][ks] = tmp[4 + ks]; kf[2][ks] = tmp[8 + ks]; qf[ks] = tmp[12 + ks]; }
    }
    f32x4 st[3];
#pragma unroll
    for (int kt = 0; kt < 3; ++kt) {
      st[kt] = (f32x4){0.f, 0.f, 0.f, 0.f};
#pragma unroll
      for (int ks = 0; ks < 4; ++ks) st[kt] = mfma16(as_frag(kf[kt][ks]), as_frag(qf[ks]), st[kt]);
    }
    const int qi = 16 * qt + l15;
    bf16x8 pb0, pb1;
    softmax_pt(st, scale, [&](int kt, int r) { const int key = 16 * kt + 4 * q + r; return key < 8 || (key < 40 && key - 8 <= qi); }, pb0, pb1);
    unsigned ok[2][2];
#pragma unroll
    for (int dd = 0; dd < 2; ++dd) {
      const int d = 16 * (2 * dq + dd) + l15;
      const char* pa = (q < 2) ? (vm + d * 16 + q * 8) : (vx + d * 64 + (4 * q - 8) * 2);
      const char* pb = vx + d * 64 + (8 + 4 * q) * 2;
      const char* pc = vx + d * 64 + (24 + 4 * (q & 1)) * 2;
      v2u a, b, c;
      asm volatile("global_load_dwordx2 %0, %3, off sc1\n\tglobal_load_dwordx2 %1, %4, off sc1\n\tglobal_load_dwordx2 %2, %5, off sc1\n\ts_waitcnt vmcnt(0)"
                   : "=&v"(a), "=&v"(b), "=&v"(c) : "v"(pa), "v"(pb), "v"(pc) : "memory");
      if (q >= 2) c = (v2u){0, 0};
      v4u f0 = {a.x, a.y, b.x, b.y}, f1 = {c.x, c.y, 0, 0};
      f32x4 o = {0.f, 0.f, 0.f, 0.f};
      o = mfma16(as_frag(f0), pb0, o);
      o = mfma16(as_frag(f1), pb1, o);
      ok[dd][0] = pack2(o[0], o[1]); ok[dd][1] = pack2(o[2], o[3]);
    }
    {
      auto r0 = __builtin_amdgcn_permlane16_swap(ok[0][0], ok[0][0], false, false);
      auto r1 = __builtin_amdgcn_permlane16_swap(ok[0][1], ok[0][1], false, false);
      auto r2 = __builtin_amdgcn_permlane16_swap(ok[1][0], ok[1][0], false, false);
      auto r3 = __builtin_amdgcn_permlane16_swap(ok[1][1], ok[1][1], false, false);
      char* dst = ws + OFF_OX + ((size_t)((seg * 2 + qt) * NH + h)) * 4096 + (2 * dq + (q & 1)) * 512 + l15 * 32 + (q >> 1) * 16;
      v4u ov = (q & 1) ? (v4u){(unsigned)r2[0], (unsigned)r3[0], (unsigned)r2[1], (unsigned)r3[1]}
                       : (v4u){(unsigned)r0[0], (unsigned)r1[0], (unsigned)r0[1], (unsigned)r1[1]};
      st16_sc1(dst, ov);
    }
    drain();
    __syncthreads();
    if (t == 0) __hip_atomic_fetch_add((unsigned*)(ws + OFF_CNTOX + seg * 64), 1u, __ATOMIC_RELAXED, __HIP_MEMORY_SCOPE_AGENT);
    STAMP(i, 1);
  }
}

DI void role_s3(const Params& P, char* smem, int c, int par) {
  const int t = threadIdx.x, lane = t & 63, w = t >> 6, q = lane >> 4, l15 = lane & 15;
  bf16x8 wb0[16], wb1[16];
  load_wo16(P.Wo, 2 * c, smem, wb0, t);
  load_wo16(P.Wo, 2 * c + 1, smem, wb1, t);
  char* ws = P.ws;
  int dead = 0;
  float* red = (float*)smem;
  for (int i = 0; i < 16; ++i) {
    const int seg = 4 * i + par;
    wait_word((const unsigned*)(ws + OFF_CNTOX + seg * 64), 31u, dead, seg == NSEG - 1);
    float* rb = red + (i & 1) * 8192 + w * 1024;
#pragma unroll
    for (int mt = 0; mt < 2; ++mt) {
      f32x4 acc0 = {0.f, 0.f, 0.f, 0.f}, acc1 = acc0;
#pragma unroll
      for (int hf = 0; hf < 2; ++hf) {
        v4u a[8];
        ld16x8s1k_sc1(ws + OFF_OX + ((size_t)((seg * 2 + mt) * NH + 4 * w)) * 4096 + hf * 8192 + (q >> 1) * 512 + l15 * 32 + (q & 1) * 16, a);
#pragma unroll
        for (int ks = 0; ks < 8; ++ks) {
          acc0 = mfma16(as_frag(a[ks]), wb0[hf * 8 + ks], acc0);
          acc1 = mfma16(as_frag(a[ks]), wb1[hf * 8 + ks], acc1);
        }
        if (seg < NSEG - 1) __builtin_amdgcn_s_sleep(S3_SLEEP);
      }
#pragma unroll
      for (int r = 0; r < 4; ++r) { rb[(16 * mt + 4 * q + r) * 32 + l15] = acc0[r]; rb[(16 * mt + 4 * q + r) * 32 + 16 + l15] = acc1[r]; }
    }
    __syncthreads();
    {
      const int row = t >> 4, cp = t & 15;
      const float* rp = red + (i & 1) * 8192 + row * 32 + 2 * cp;
      float x0 = 0.f, x1 = 0.f;
#pragma unroll
      for (int ww = 0; ww < 8; ++ww) { x0 += rp[ww * 1024]; x1 += rp[ww * 1024 + 1]; }
      *(unsigned*)(P.out + (size_t)(seg * 32 + row) * OUTW + 32 * c + 2 * cp) = pack2(x0, x1);
    }
    STAMP(i, 0);
  }
}

constexpr int PB_A = 0;
constexpr int PB_R = 2 * 16 * AST;
constexpr int RST = 496;
constexpr int PB_O = PB_R + 3 * 16 * RST;
constexpr int OST = 176;
constexpr int PB_STG = 81920;
DI void role_pb(const Params& P, char* smem, int g, int rb) {
  const int t = threadIdx.x, lane = t & 63, w = t >> 6, q = lane >> 4, l15 = lane & 15;
  char* ws = P.ws;
  int dead = 0;
  const int gg = lane >> 4, qq = (lane & 15) >> 2, pp = lane & 3;
  {
    bf16x8 wb[40];
#pragma unroll
    for (int nn = 0; nn < 2; ++nn) {
      const int n = (2 * w + nn) < 15 ? (2 * w + nn) : 14;
      const int col0 = (n / 5) * DM + 80 * g + 16 * (n % 5);
      const u16* src = P.wqkv + (size_t)(lane >> 1) * 1920 + col0 + 8 * (lane & 1);
#pragma unroll
      for (int half = 0; half < 2; ++half) {
        v4u tmp[10];
#pragma unroll
        for (int i = 0; i < 10; ++i) tmp[i] = *(const v4u*)(src + (size_t)(half * 10 + i) * 32 * 1920);
        char* base = smem + PB_STG + w * 10240;
#pragma unroll
        for (int i = 0; i < 10; ++i) *(v4u*)(base + i * 1024 + lane * 16) = tmp[i];
        __syncthreads();
#pragma unroll
        for (int i = 0; i < 10; ++i) {
          s16x4 lo = tr_read(base + i * 1024 + (8 * gg + qq) * 32 + pp * 8);
          s16x4 hi = tr_read(base + i * 1024 + (8 * gg + 4 + qq) * 32 + pp * 8);
          wb[nn * 20 + half * 10 + i] = cat4(lo, hi);
        }
        __syncthreads();
      }
    }
    v4u pa[3];
    auto fetchA = [&](int j) {
      const int row0 = 512 * rb - 16 + 16 * j;
      if (row0 < 0) { pa[0] = (v4u){0, 0, 0, 0}; pa[1] = pa[0]; pa[2] = pa[0]; return; }
      const char* src = (const char*)P.bx + (size_t)row0 * (DM * 2);
      pa[0] = *(const v4u*)(src + t * 16);
      pa[1] = *(const v4u*)(src + (512 + t) * 16);
      if (t < 256) pa[2] = *(const v4u*)(src + (1024 + t) * 16);
    };
    auto putA = [&](int buf) {
      char* dst = smem + PB_A + buf * 16 * AST;
      *(v4u*)(dst + (t / 80) * AST + (t % 80) * 16) = pa[0];
      *(v4u*)(dst + ((512 + t) / 80) * AST + ((512 + t) % 80) * 16) = pa[1];
      if (t < 256) *(v4u*)(dst + ((1024 + t) / 80) * AST + ((1024 + t) % 80) * 16) = pa[2];
    };
    fetchA(0); putA(0); fetchA(1);
    __syncthreads();
    const float scale = 0.22360679774997896f;
    for (int j = 0; j < 33; ++j) {
      const char* ap = smem + PB_A + (j & 1) * 16 * AST + l15 * AST + q * 16;
      f32x4 c0 = {0.f, 0.f, 0.f, 0.f}, c1 = c0;
#pragma unroll
      for (int ks = 0; ks < 20; ++ks) {
        bf16x8 a = lds_frag(ap + ks * 64);
        c0 = mfma16(a, wb[ks], c0);
        c1 = mfma16(a, wb[20 + ks], c1);
      }
      char* R = smem + PB_R + (j % 3) * 16 * RST;
#pragma unroll
      for (int r = 0; r < 4; ++r) {
        *(u16*)(R + (4 * q + r) * RST + (32 * w + l15) * 2) = f2bf(c0[r]);
        if (w < 7) *(u16*)(R + (4 * q + r) * RST + (32 * w + 16 + l15) * 2) = f2bf(c1[r]);
      }
      if (j + 1 < 33) { putA((j + 1) & 1); if (j + 2 < 33) fetchA(j + 2); }
      __syncthreads();
      if (j >= 2 && t < 160) {
        const int row = t / 10, ch = t % 10;
        v4u v = *(const v4u*)(smem + PB_O + ((j - 1) & 1) * 16 * OST + row * OST + ch * 16);
        st16_sc1(ws + OFF_OB + (size_t)(512 * rb - 16 + 16 * (j - 1) + row) * 1280 + (80 * g) * 2 + ch * 16, v);
      }
      if (j >= 1) {
        const int item = t >> 3, wi = t & 7, row = item >> 2, hh = item & 3;
        const int srow = 512 * rb - 16 + 16 * j + row;
        const int krow = row - 2 * wi;
        const bool vis = (srow - 2 * wi) >= 0;
        const char* Rc = smem + PB_R + (j % 3) * 16 * RST;
        const char* Rp = smem + PB_R + ((j + 2) % 3) * 16 * RST;
        const char* qrow = Rc + row * RST + (20 * hh) * 2;
        const char* krowp = (krow >= 0 ? Rc + krow * RST : Rp + (krow + 16) * RST);
        float dot = 0.f;
#pragma unroll
        for (int d = 0; d < 20; d += 2) {
          unsigned qv = *(const unsigned*)(qrow + d * 2);
          unsigned kv = *(const unsigned*)(krowp + (80 + 20 * hh + d) * 2);
          dot += bf2f((u16)qv) * bf2f((u16)kv);
          dot += bf2f((u16)(qv >> 16)) * bf2f((u16)(kv >> 16));
        }
        float lg = rbf(rbf(dot) * scale);
        float mx = vis ? lg : -3.0e38f;
        mx = fmaxf(mx, __shfl_xor(mx, 1)); mx = fmaxf(mx, __shfl_xor(mx, 2)); mx = fmaxf(mx, __shfl_xor(mx, 4));
        float e = vis ? __expf(lg - mx) : 0.f;
        float sum = e;
        sum += __shfl_xor(sum, 1); sum += __shfl_xor(sum, 2); sum += __shfl_xor(sum, 4);
        const float pw = rbf(e * (1.0f / sum));
        float o0 = 0.f, o1 = 0.f, o2 = 0.f;
#pragma unroll
        for (int ww = 0; ww < 8; ++ww) {
          const float pv = __shfl(pw, (lane & ~7) + ww);
          const int kr = row - 2 * ww;
          const char* vr = (kr >= 0 ? Rc + kr * RST : Rp + (kr + 16) * RST) + (160 + 20 * hh) * 2;
          o0 += pv * bf2f(*(const u16*)(vr + wi * 2));
          o1 += pv * bf2f(*(const u16*)(vr + (wi + 8) * 2));
          if (wi < 4) o2 += pv * bf2f(*(const u16*)(vr + (wi + 16) * 2));
        }
        char* O = smem + PB_O + (j & 1) * 16 * OST + row * OST + (20 * hh) * 2;
        *(u16*)(O + wi * 2) = f2bf(o0);
        *(u16*)(O + (wi + 8) * 2) = f2bf(o1);
        if (wi < 4) *(u16*)(O + (wi + 16) * 2) = f2bf(o2);
      }
    }
    __syncthreads();
    if (t < 160) {
      const int row = t / 10, ch = t % 10;
      v4u v = *(const v4u*)(smem + PB_O + (32 & 1) * 16 * OST + row * OST + ch * 16);
      st16_sc1(ws + OFF_OB + (size_t)(512 * rb - 16 + 16 * 32 + row) * 1280 + (80 * g) * 2 + ch * 16, v);
    }
    drain();
    __syncthreads();
    if (t == 0) __hip_atomic_fetch_add((unsigned*)(ws + OFF_CNTB + rb * 64), 1u, __ATOMIC_RELAXED, __HIP_MEMORY_SCOPE_AGENT);
  }
  {
    char* stg = smem + 102400 + w * 7168;
    for (int f0 = w * 13; f0 < w * 13 + 13 && f0 < 100; f0 += 7) {
      v4u tmp[7];
#pragma unroll
      for (int i = 0; i < 7; ++i) {
        const int f = f0 + i; const bool okf = (f < w * 13 + 13) && (f < 100);
        const int ff = okf ? f : 0;
        tmp[i] = *(const v4u*)(P.wo + (size_t)((ff % 20) * 32 + (lane >> 1)) * DM + 80 * g + 16 * (ff / 20) + 8 * (lane & 1));
      }
#pragma unroll
      for (int i = 0; i < 7; ++i) *(v4u*)(stg + i * 1024 + lane * 16) = tmp[i];
      __builtin_amdgcn_s_waitcnt(0xC07F);
#pragma unroll
      for (int i = 0; i < 7; ++i) {
        const int f = f0 + i; const bool okf = (f < w * 13 + 13) && (f < 100);
        s16x4 lo = tr_read(stg + i * 1024 + (8 * gg + qq) * 32 + pp * 8);
        s16x4 hi = tr_read(stg + i * 1024 + (8 * gg + 4 + qq) * 32 + pp * 8);
        if (okf) *(bf16x8*)(smem + f * 1024 + lane * 16) = cat4(lo, hi);
      }
      __builtin_amdgcn_s_waitcnt(0xC07F);
    }
    __syncthreads();
    wait_word((const unsigned*)(ws + OFF_CNTB + rb * 64), 7u, dead);
    for (int i = 0; i < 4; ++i) {
      const int row0 = 512 * rb + 16 * (w + 8 * i);
      v4u a0[10], a1[10];
      ld16x10_sc1(ws + OFF_OB + (size_t)(row0 + l15) * 1280 + q * 16, a0);
      ld16x10_sc1(ws + OFF_OB + (size_t)(row0 + l15) * 1280 + 640 + q * 16, a1);
#pragma unroll
      for (int n = 0; n < 5; ++n) {
        f32x4 acc = {0.f, 0.f, 0.f, 0.f};
#pragma unroll
        for (int ks = 0; ks < 10; ++ks) acc = mfma16(as_frag(a0[ks]), lds_frag(smem + (n * 20 + ks) * 1024 + lane * 16), acc);
#pragma unroll
        for (int ks = 0; ks < 10; ++ks) acc = mfma16(as_frag(a1[ks]), lds_frag(smem + (n * 20 + 10 + ks) * 1024 + lane * 16), acc);
#pragma unroll
        for (int r = 0; r < 4; ++r) P.out[(size_t)(row0 + 4 * q + r) * OUTW + DM + 80 * g + 16 * n + l15] = f2bf(acc[r]);
      }
    }
  }
}

__global__ __launch_bounds__(512, 2) void fused_kernel(Params P) {
  extern __shared__ __attribute__((aligned(16))) char smem[];
  const int b = blockIdx.x;
  STAMP(63, 7);
  if (b < 32) role_s1(P, smem, b);
  else if (b < 72) role_s2(P, smem, b - 32);
  else if (b < 136) role_s4(P, smem, (b - 72) & 31, (b - 72) >> 5);
  else if (b < 216) role_s3(P, smem, (b - 136) % 20, (b - 136) / 20);
  else if (b < 248) role_pb(P, smem, (b - 216) & 7, (b - 216) >> 3);
  STAMP(63, 6);
}

extern "C" void kernel_launch(void* const* d_in, const int* in_sizes, int n_in,
                              void* d_out, int out_size, void* d_ws, size_t ws_size,
                              hipStream_t stream) {
  Params P;
  P.ax = (const u16*)d_in[0]; P.mem0 = (const u16*)d_in[1];
  P.Wq = (const u16*)d_in[2]; P.Wk = (const u16*)d_in[3]; P.Wv = (const u16*)d_in[4]; P.Wo = (const u16*)d_in[5];
  P.bx = (const u16*)d_in[7]; P.wqkv = (const u16*)d_in[8]; P.wo = (const u16*)d_in[9];
  P.out = (u16*)d_out; P.ws = (char*)d_ws;
  hipFuncSetAttribute((const void*)fused_kernel, hipFuncAttributeMaxDynamicSharedMemorySize, LDS_BYTES);
  hipMemsetAsync(d_ws, 0xFF, POISON_BYTES, stream);
  fused_kernel<<<256, 512, LDS_BYTES, stream>>>(P);
}
```

```cpp
#include <hip/hip_runtime.h>
#include <hip/hip_bf16.h>
#include <stdint.h>

typedef unsigned short u16;
typedef short bf16x8 __attribute__((ext_vector_type(8)));
typedef short s16x4 __attribute__((ext_vector_type(4)));
typedef float f32x4 __attribute__((ext_vector_type(4)));
typedef unsigned v4u __attribute__((ext_vector_type(4)));
typedef unsigned v2u __attribute__((ext_vector_type(2)));
#define LAS __attribute__((address_space(3)))
#define DI __device__ __forceinline__

constexpr int SLEN = 2048, DM = 640, NH = 32, DH = 128, HD = 4096, NSEG = 64;
constexpr int OUTW = 1280;
constexpr size_t OFF_FLAGKV = 0;
constexpr size_t OFF_FLAGM  = 8192;
constexpr size_t OFF_CNTOX  = 16384;
constexpr size_t OFF_CNTB   = 20480;
constexpr size_t OFF_MEMBUF = 24576;
constexpr size_t OFF_OBUF   = OFF_MEMBUF + 64 * 10240;
constexpr size_t POISON_BYTES = OFF_OBUF + (size_t)64 * 65536;
constexpr size_t OFF_KX  = (size_t)8 << 20;
constexpr size_t OFF_VXT = (size_t)24 << 20;
constexpr size_t OFF_QX  = (size_t)40 << 20;
constexpr size_t OFF_KM  = (size_t)56 << 20;
constexpr size_t OFF_VMT = (size_t)60 << 20;
constexpr size_t OFF_OX  = (size_t)64 << 20;
constexpr size_t OFF_OB  = (size_t)80 << 20;
constexpr unsigned SENT = 0xFFFFFFFFu;
constexpr int SPIN_CAP = 300000;
constexpr int LDS_BYTES = 160 * 1024;
#define POLL_SLEEP 60
#define S1_PRE 28
#define S2_PRE 76
#define S1_POST 0
#define S2_POST 0
#define S3_SLEEP 20

#define STAMP(s, i) do {} while (0)
DI float bf2f(u16 b) { return __builtin_bit_cast(float, (unsigned)b << 16); }
DI u16 f2bf(float f) { __bf16 b = (__bf16)f; return __builtin_bit_cast(u16, b); }
DI float rbf(float f) { return bf2f(f2bf(f)); }
DI unsigned pack2(float a, float b) { return (unsigned)f2bf(a) | ((unsigned)f2bf(b) << 16); }
DI unsigned unsent(unsigned x) { return x == SENT ? 0xFFFEFFFFu : x; }
DI bool valid4(v4u v) { return v.x != SENT && v.y != SENT && v.z != SENT && v.w != SENT; }
DI f32x4 mfma16(bf16x8 a, bf16x8 b, f32x4 c) { return __builtin_amdgcn_mfma_f32_16x16x32_bf16(a, b, c, 0, 0, 0); }
DI bf16x8 as_frag(v4u v) { return __builtin_bit_cast(bf16x8, v); }
DI s16x4 tr_read(const char* p) {
  typedef short v4i16_t __attribute__((ext_vector_type(4)));
  return __builtin_amdgcn_ds_read_tr16_b64_v4i16((LAS v4i16_t*)(p));
}
DI float red_max(float x) {
  unsigned u = __builtin_bit_cast(unsigned, x);
  auto r = __builtin_amdgcn_permlane16_swap(u, u, false, false);
  x = fmaxf(__builtin_bit_cast(float, (unsigned)r[0]), __builtin_bit_cast(float, (unsigned)r[1]));
  u = __builtin_bit_cast(unsigned, x);
  auto r2 = __builtin_amdgcn_permlane32_swap(u, u, false, false);
  return fmaxf(__builtin_bit_cast(float, (unsigned)r2[0]), __builtin_bit_cast(float, (unsigned)r2[1]));
}
DI float red_sum(float x) {
  unsigned u = __builtin_bit_cast(unsigned, x);
  auto r = __builtin_amdgcn_permlane16_swap(u, u, false, false);
  x = __builtin_bit_cast(float, (unsigned)r[0]) + __builtin_bit_cast(float, (unsigned)r[1]);
  u = __builtin_bit_cast(unsigned, x);
  auto r2 = __builtin_amdgcn_permlane32_swap(u, u, false, false);
  return __builtin_bit_cast(float, (unsigned)r2[0]) + __builtin_bit_cast(float, (unsigned)r2[1]);
}
DI bf16x8 cat4(s16x4 lo, s16x4 hi) { bf16x8 r; r[0]=lo[0]; r[1]=lo[1]; r[2]=lo[2]; r[3]=lo[3]; r[4]=hi[0]; r[5]=hi[1]; r[6]=hi[2]; r[7]=hi[3]; return r; }
DI bf16x8 lds_frag(const char* p) { return *(const bf16x8*)p; }

DI v4u ld16_sc1(const void* p) { v4u r; asm volatile("global_load_dwordx4 %0, %1, off sc1\n\ts_waitcnt vmcnt(0)" : "=&v"(r) : "v"(p) : "memory"); return r; }
DI void ld16x2_sc1(const void* p0, const void* p1, v4u& a, v4u& b) {
  asm volatile("global_load_dwordx4 %0, %2, off sc1\n\tglobal_load_dwordx4 %1, %3, off sc1\n\ts_waitcnt vmcnt(0)"
               : "=&v"(a), "=&v"(b) : "v"(p0), "v"(p1) : "memory");
}
DI unsigned ld4_sc1(const void* p) { unsigned r; asm volatile("global_load_dword %0, %1, off sc1\n\ts_waitcnt vmcnt(0)" : "=&v"(r) : "v"(p) : "memory"); return r; }
DI void st16_sc1(void* p, v4u v) { asm volatile("global_store_dwordx4 %0, %1, off sc1\n\ts_nop 1" :: "v"(p), "v"(v) : "memory"); }
DI void st8_sc1(void* p, v2u v) { asm volatile("global_store_dwordx2 %0, %1, off sc1\n\ts_nop 1" :: "v"(p), "v"(v) : "memory"); }
DI void st4_sc1(void* p, unsigned v) { asm volatile("global_store_dword %0, %1, off sc1\n\ts_nop 1" :: "v"(p), "v"(v) : "memory"); }
DI float dpp_xor1(float x) { return __builtin_bit_cast(float, __builtin_amdgcn_update_dpp(0, __builtin_bit_cast(int, x), 0xB1, 0xf, 0xf, true)); }
DI float dpp_xor2(float x) { return __builtin_bit_cast(float, __builtin_amdgcn_update_dpp(0, __builtin_bit_cast(int, x), 0x4E, 0xf, 0xf, true)); }
DI float dpp_shl4(float x) { return __builtin_bit_cast(float, __builtin_amdgcn_update_dpp(0, __builtin_bit_cast(int, x), 0x104, 0xf, 0xf, true)); }
template <int CTRL> DI float dpp_ror(float x) { return __builtin_bit_cast(float, __builtin_amdgcn_update_dpp(0, __builtin_bit_cast(int, x), CTRL, 0xf, 0xf, true)); }
DI float row_sum16(float x) { x += dpp_ror<0x128>(x); x += dpp_ror<0x124>(x); x += dpp_ror<0x122>(x); x += dpp_ror<0x121>(x); return x; }
DI void drain() { asm volatile("s_waitcnt vmcnt(0)" ::: "memory"); }
DI void ld16x8s1k_sc1(const char* p, v4u (&a)[8]) {
  asm volatile(
    "global_load_dwordx4 %0, %8, off sc1\n\t"
    "global_load_dwordx4 %1, %8, off offset:1024 sc1\n\t"
    "global_load_dwordx4 %2, %8, off offset:2048 sc1\n\t"
    "global_load_dwordx4 %3, %8, off offset:3072 sc1\n\t"
    "global_load_dwordx4 %4, %9, off sc1\n\t"
    "global_load_dwordx4 %5, %9, off offset:1024 sc1\n\t"
    "global_load_dwordx4 %6, %9, off offset:2048 sc1\n\t"
    "global_load_dwordx4 %7, %9, off offset:3072 sc1\n\t"
    "s_waitcnt vmcnt(0)"
    : "=&v"(a[0]), "=&v"(a[1]), "=&v"(a[2]), "=&v"(a[3]), "=&v"(a[4]), "=&v"(a[5]), "=&v"(a[6]), "=&v"(a[7])
    : "v"(p), "v"(p + 4096) : "memory");
}
DI void ld16x8s512_sc1(const char* p, v4u (&a)[8]) {
  asm volatile(
    "global_load_dwordx4 %0, %8, off sc1\n\t"
    "global_load_dwordx4 %1, %8, off offset:512 sc1\n\t"
    "global_load_dwordx4 %2, %8, off offset:1024 sc1\n\t"
    "global_load_dwordx4 %3, %8, off offset:1536 sc1\n\t"
    "global_load_dwordx4 %4, %8, off offset:2048 sc1\n\t"
    "global_load_dwordx4 %5, %8, off offset:2560 sc1\n\t"
    "global_load_dwordx4 %6, %8, off offset:3072 sc1\n\t"
    "global_load_dwordx4 %7, %8, off offset:3584 sc1\n\t"
    "s_waitcnt vmcnt(0)"
    : "=&v"(a[0]), "=&v"(a[1]), "=&v"(a[2]), "=&v"(a[3]), "=&v"(a[4]), "=&v"(a[5]), "=&v"(a[6]), "=&v"(a[7])
    : "v"(p) : "memory");
}
DI void ld16x10_sc1(const char* p, v4u (&a)[10]) {
  asm volatile(
    "global_load_dwordx4 %0, %10, off sc1\n\t"
    "global_load_dwordx4 %1, %10, off offset:64 sc1\n\t"
    "global_load_dwordx4 %2, %10, off offset:128 sc1\n\t"
    "global_load_dwordx4 %3, %10, off offset:192 sc1\n\t"
    "global_load_dwordx4 %4, %10, off offset:256 sc1\n\t"
    "global_load_dwordx4 %5, %10, off offset:320 sc1\n\t"
    "global_load_dwordx4 %6, %10, off offset:384 sc1\n\t"
    "global_load_dwordx4 %7, %10, off offset:448 sc1\n\t"
    "global_load_dwordx4 %8, %10, off offset:512 sc1\n\t"
    "global_load_dwordx4 %9, %10, off offset:576 sc1\n\t"
    "s_waitcnt vmcnt(0)"
    : "=&v"(a[0]), "=&v"(a[1]), "=&v"(a[2]), "=&v"(a[3]), "=&v"(a[4]), "=&v"(a[5]), "=&v"(a[6]), "=&v"(a[7]),
      "=&v"(a[8]), "=&v"(a[9])
    : "v"(p) : "memory");
}
DI void wait_word(const unsigned* p, unsigned want, int& dead, bool fast = false) {
  if ((threadIdx.x >> 6) == 0 && !dead) {
    int sp = 0;
    for (; sp < SPIN_CAP; ++sp) {
      unsigned v = ld4_sc1(p);
      if (__builtin_amdgcn_readfirstlane(v) == want) break;
      if (fast) __builtin_amdgcn_s_sleep(4); else __builtin_amdgcn_s_sleep(POLL_SLEEP);
    }
    if (sp >= SPIN_CAP) dead = 1;
  }
  __syncthreads();
}

constexpr int AST = 1296;
constexpr int KST = 272;
constexpr int VST = 136;
constexpr int L_WRES = 0;
constexpr int L_STG = 98304;
constexpr int STG_BUF = 3 * 32 * KST;
constexpr int S1_A = 98304;
constexpr int S1_K = S1_A + 10496;
constexpr int S1_VT = S1_K + 48 * KST;
constexpr int S1_Q = S1_VT + 128 * VST;
constexpr int S4_A = 98304;
constexpr int S4_T = S4_A + 2 * 16 * AST;
constexpr int TST = 528;

DI void load_w3(const u16* __restrict__ Wq, const u16* __restrict__ Wk, const u16* __restrict__ Wv, int h,
                char* smem, bf16x8 (&wr)[48], int t) {
  const int lane = t & 63, w = t >> 6;
  const int r = t >> 4, c16 = t & 15;
  const size_t goff = (size_t)r * HD + h * DH + c16 * 8;
  const int g = lane >> 4, qq = (lane & 15) >> 2, p = lane & 3;
  const int troff = (8 * g + qq) * KST + (16 * w + 4 * p) * 2;
  v4u pf[4][3];
#pragma unroll
  for (int i = 0; i < 4; ++i) {
    pf[i][0] = *(const v4u*)(Wq + goff + (size_t)i * 32 * HD);
    pf[i][1] = *(const v4u*)(Wk + goff + (size_t)i * 32 * HD);
    pf[i][2] = *(const v4u*)(Wv + goff + (size_t)i * 32 * HD);
  }
#pragma unroll
  for (int ks = 0; ks < 20; ++ks) {
    char* stg = smem + L_STG + (ks & 1) * STG_BUF;
#pragma unroll
    for (int m = 0; m < 3; ++m) *(v4u*)(stg + m * 32 * KST + r * KST + c16 * 16) = pf[ks & 3][m];
    if (ks + 4 < 20) {
      pf[ks & 3][0] = *(const v4u*)(Wq + goff + (size_t)(ks + 4) * 32 * HD);
      pf[ks & 3][1] = *(const v4u*)(Wk + goff + (size_t)(ks + 4) * 32 * HD);
      pf[ks & 3][2] = *(const v4u*)(Wv + goff + (size_t)(ks + 4) * 32 * HD);
    }
    __syncthreads();
#pragma unroll
    for (int m = 0; m < 3; ++m) {
      s16x4 lo = tr_read(stg + m * 32 * KST + troff);
      s16x4 hi = tr_read(stg + m * 32 * KST + troff + 4 * KST);
      bf16x8 f = cat4(lo, hi);
      if (ks < 16) wr[ks * 3 + m] = f;
      else *(bf16x8*)(smem + L_WRES + w * 12288 + ((ks - 16) * 3 + m) * 1024 + lane * 16) = f;
    }
  }
  __syncthreads();
}

DI void proj3(const char* ldsA, int arow, const char* smem, const bf16x8 (&wr)[48], int t, f32x4& aq, f32x4& ak, f32x4& av) {
  const int lane = t & 63, w = t >> 6, q = lane >> 4;
  const char* ap = ldsA + arow * AST + q * 16;
  int woff = w * 12288 + lane * 16;
  asm volatile("" : "+v"(woff));
  const char* wp = smem + L_WRES + woff;
  aq = (f32x4){0.f, 0.f, 0.f, 0.f}; ak = aq; av = aq;
#pragma unroll
  for (int ks = 0; ks < 20; ++ks) {
    bf16x8 a = lds_frag(ap + ks * 64);
    bf16x8 b0, b1, b2;
    if (ks < 16) { b0 = wr[ks * 3]; b1 = wr[ks * 3 + 1]; b2 = wr[ks * 3 + 2]; }
    else { b0 = lds_frag(wp + ((ks - 16) * 3) * 1024); b1 = lds_frag(wp + ((ks - 16) * 3 + 1) * 1024); b2 = lds_frag(wp + ((ks - 16) * 3 + 2) * 1024); }
    aq = mfma16(a, b0, aq);
    ak = mfma16(a, b1, ak);
    av = mfma16(a, b2, av);
    if ((ks & 3) == 3) { asm volatile("" : "+v"(aq), "+v"(ak), "+v"(av)); __builtin_amdgcn_sched_barrier(0); }
  }
}

template <class VIS>
DI void softmax_pt(const f32x4 (&st)[3], float scale, VIS vis, bf16x8& pb0, bf16x8& pb1) {
  float s[3][4]; float mx = -3.0e38f;
#pragma unroll
  for (int kt = 0; kt < 3; ++kt)
#pragma unroll
    for (int r = 0; r < 4; ++r) {
      float v = rbf(rbf(st[kt][r]) * scale);
      s[kt][r] = v;
      if (vis(kt, r)) mx = fmaxf(mx, v);
    }
  mx = red_max(mx);
  float sum = 0.f;
#pragma unroll
  for (int kt = 0; kt < 3; ++kt)
#pragma unroll
    for (int r = 0; r < 4; ++r) {
      float e = vis(kt, r) ? __expf(s[kt][r] - mx) : 0.f;
      s[kt][r] = e; sum += e;
    }
  sum = red_sum(sum);
  const float inv = 1.0f / sum;
#pragma unroll
  for (int r = 0; r < 4; ++r) {
    pb0[r] = (short)f2bf(s[0][r] * inv);
    pb0[4 + r] = (short)f2bf(s[1][r] * inv);
    pb1[r] = (short)f2bf(s[2][r] * inv);
    pb1[4 + r] = 0;
  }
}

struct Params {
  const u16* ax; const u16* mem0; const u16* Wq; const u16* Wk; const u16* Wv; const u16* Wo;
  const u16* bx; const u16* wqkv; const u16* wo;
  u16* out; char* ws;
};

DI void role_s1(const Params& P, char* smem, int h) {
  int t = threadIdx.x;
  bf16x8 wr[48];
  load_w3(P.Wq, P.Wk, P.Wv, h, smem, wr, t);
  char* ws = P.ws;
  int dead = 0;
  for (int i = t; i < 8 * 16; i += 512) *(v4u*)(smem + S1_K + (40 + (i >> 4)) * KST + (i & 15) * 16) = (v4u){0, 0, 0, 0};
  for (int i = t; i < 128 * 6; i += 512) *(v2u*)(smem + S1_VT + (i / 6) * VST + 80 + (i % 6) * 8) = (v2u){0, 0};
  const float scale = 0.08838834764831845f;
  for (int s = 0; s < NSEG; ++s) {
    asm volatile("" : "+v"(t));
    const int lane = t & 63, w = t >> 6, q = lane >> 4, l15 = lane & 15;
    auto fetch_kv = [&]() {
      wait_word((const unsigned*)(ws + OFF_FLAGKV) + h * 64 + s, 1u, dead, s == 0);
      const char* kx = ws + OFF_KX + ((size_t)(s * NH + h)) * 8192;
      const char* vx = ws + OFF_VXT + ((size_t)(s * NH + h)) * 8192;
      v4u a, b;
      ld16x2_sc1(kx + t * 16, vx + t * 16, a, b);
      *(v4u*)(smem + S1_K + (8 + (t >> 4)) * KST + (t & 15) * 16) = a;
      v2u b0 = {b.x, b.y}, b1 = {b.z, b.w};
      *(v2u*)(smem + S1_VT + (t >> 2) * VST + 16 + (t & 3) * 16) = b0;
      *(v2u*)(smem + S1_VT + (t >> 2) * VST + 16 + (t & 3) * 16 + 8) = b1;
    };
    if (s > 0) fetch_kv();
    STAMP(s, 0);
    {
      const int c0 = t, c1 = 512 + t;
      v4u a, b = {0, 0, 0, 0};
      if (s == 0) {
        a = *(const v4u*)((const char*)P.mem0 + c0 * 16);
        if (w < 2) b = *(const v4u*)((const char*)P.mem0 + c1 * 16);
      } else {
        const char* src = ws + OFF_MEMBUF + (size_t)s * 10240;
        if (S1_PRE > 0) { __builtin_amdgcn_s_sleep(S1_PRE / 2); __builtin_amdgcn_s_sleep(S1_PRE - S1_PRE / 2); }
        {
          const char* prb = src + ((w * 10 + (h % 10)) * 128 + 124);
          for (int sp = 0;; ++sp) {
            unsigned pv = ld4_sc1(prb);
            if (__builtin_amdgcn_readfirstlane(pv) != SENT) break;
            if (dead || sp > SPIN_CAP) { dead = 1; break; }
          }
          if (S1_POST > 0) __builtin_amdgcn_s_sleep(S1_POST);
        }
        for (int sp = 0;; ++sp) {
          bool ok;
          if (w < 2) { ld16x2_sc1(src + c0 * 16, src + c1 * 16, a, b); ok = valid4(a) && valid4(b); }
          else { a = ld16_sc1(src + c0 * 16); ok = valid4(a); }
          if (__all(ok)) break;
          if (dead || sp > SPIN_CAP) { dead = 1; break; }
        }
      }
      if (s == 0) {
        *(v4u*)(smem + S1_A + (c0 / 80) * AST + (c0 % 80) * 16) = a;
        if (w < 2) *(v4u*)(smem + S1_A + (c1 / 80) * AST + (c1 % 80) * 16) = b;
      } else {
        *(v4u*)(smem + S1_A + ((c0 >> 1) & 7) * AST + (c0 >> 4) * 32 + (c0 & 1) * 16) = a;
        if (w < 2) *(v4u*)(smem + S1_A + ((c1 >> 1) & 7) * AST + (c1 >> 4) * 32 + (c1 & 1) * 16) = b;
      }
    }
    __syncthreads();
    STAMP(s, 1);
    f32x4 aq, ak, av;
    proj3(smem + S1_A, lane & 7, smem, wr, t, aq, ak, av);
    if (q < 2) {
#pragma unroll
      for (int r = 0; r < 4; ++r) {
        *(u16*)(smem + S1_Q + (4 * q + r) * KST + (16 * w + l15) * 2) = f2bf(aq[r]);
        *(u16*)(smem + S1_K + (4 * q + r) * KST + (16 * w + l15) * 2) = f2bf(ak[r]);
      }
      v2u vv = {pack2(av[0], av[1]), pack2(av[2], av[3])};
      *(v2u*)(smem + S1_VT + (16 * w + l15) * VST + q * 8) = vv;
    }
    if (s == 0) fetch_kv();
    __syncthreads();
    STAMP(s, 2);
    if (s < NSEG - 1 && w < 4) {
      const char* qp = smem + S1_Q + (lane & 7) * KST + q * 16;
      const char* kp = smem + S1_K + l15 * KST + q * 16;
      f32x4 st[3];
      st[0] = (f32x4){0.f, 0.f, 0.f, 0.f}; st[1] = st[0]; st[2] = st[0];
      bf16x8 qf0 = lds_frag(qp), ka0 = lds_frag(kp), kb0 = lds_frag(kp + 16 * KST), kc0 = lds_frag(kp + 32 * KST);
      bf16x8 qf1 = lds_frag(qp + 64), ka1 = lds_frag(kp + 64), kb1 = lds_frag(kp + 16 * KST + 64), kc1 = lds_frag(kp + 32 * KST + 64);
      st[0] = mfma16(ka0, qf0, st[0]); st[1] = mfma16(kb0, qf0, st[1]); st[2] = mfma16(kc0, qf0, st[2]);
      qf0 = lds_frag(qp + 128); ka0 = lds_frag(kp + 128); kb0 = lds_frag(kp + 16 * KST + 128); kc0 = lds_frag(kp + 32 * KST + 128);
      st[0] = mfma16(ka1, qf1, st[0]); st[1] = mfma16(kb1, qf1, st[1]); st[2] = mfma16(kc1, qf1, st[2]);
      qf1 = lds_frag(qp + 192); ka1 = lds_frag(kp + 192); kb1 = lds_frag(kp + 16 * KST + 192); kc1 = lds_frag(kp + 32 * KST + 192);
      st[0] = mfma16(ka0, qf0, st[0]); st[1] = mfma16(kb0, qf0, st[1]); st[2] = mfma16(kc0, qf0, st[2]);
      const char* vp = smem + S1_VT + (32 * w + l15) * VST + q * 8;
      s16x4 v0 = *(const s16x4*)(vp), v1 = *(const s16x4*)(vp + 32), v2 = *(const s16x4*)(vp + 64);
      s16x4 u0 = *(const s16x4*)(vp + 16 * VST), u1 = *(const s16x4*)(vp + 16 * VST + 32), u2 = *(const s16x4*)(vp + 16 * VST + 64);
      st[0] = mfma16(ka1, qf1, st[0]); st[1] = mfma16(kb1, qf1, st[1]); st[2] = mfma16(kc1, qf1, st[2]);
      bf16x8 pb0, pb1;
      softmax_pt(st, scale, [&](int kt, int r) { return kt < 2 || q < 2; }, pb0, pb1);
      const s16x4 z4 = {0, 0, 0, 0};
      f32x4 o = {0.f, 0.f, 0.f, 0.f}, o2 = o;
      o = mfma16(cat4(v0, v1), pb0, o);
      o2 = mfma16(cat4(u0, u1), pb0, o2);
      o = mfma16(cat4(v2, z4), pb1, o);
      o2 = mfma16(cat4(u2, z4), pb1, o2);
      {
        unsigned o00 = unsent(pack2(o[0], o[1])), o01 = unsent(pack2(o[2], o[3]));
        unsigned o10 = unsent(pack2(o2[0], o2[1])), o11 = unsent(pack2(o2[2], o2[3]));
        auto r0 = __builtin_amdgcn_permlane16_swap(o00, o00, false, false);
        auto r1 = __builtin_amdgcn_permlane16_swap(o01, o01, false, false);
        auto r2 = __builtin_amdgcn_permlane16_swap(o10, o10, false, false);
        auto r3 = __builtin_amdgcn_permlane16_swap(o11, o11, false, false);
        if (l15 < 8) {
          char* dst = ws + OFF_OBUF + (size_t)s * 65536 + h * 2048 + (2 * w + (q & 1)) * 256 + l15 * 32 + (q >> 1) * 16;
          v4u ov = (q & 1) ? (v4u){(unsigned)r2[0], (unsigned)r3[0], (unsigned)r2[1], (unsigned)r3[1]}
                           : (v4u){(unsigned)r0[0], (unsigned)r1[0], (unsigned)r0[1], (unsigned)r1[1]};
          st16_sc1(dst, ov);
        }
      }
    }
    STAMP(s, 3);
    if (t < 128) {
      v4u kv = *(const v4u*)(smem + S1_K + (t >> 4) * KST + (t & 15) * 16);
      st16_sc1(ws + OFF_KM + ((size_t)(s * NH + h)) * 2048 + t * 16, kv);
    } else if (t < 256) {
      const int d = t - 128;
      v2u a = *(const v2u*)(smem + S1_VT + d * VST), b = *(const v2u*)(smem + S1_VT + d * VST + 8);
      v4u vv = {a.x, a.y, b.x, b.y};
      st16_sc1(ws + OFF_VMT + ((size_t)(s * NH + h)) * 2048 + d * 16, vv);
    }
    drain();
    __syncthreads();
    if (t == 0) st4_sc1((unsigned*)(ws + OFF_FLAGM) + h * 64 + s, 1u);
    STAMP(s, 4);
  }
}

DI void load_wo16(const u16* __restrict__ Wo, int c, char* smem, bf16x8 (&wb)[16], int t) {
  const int lane = t & 63, w = t >> 6;
  const u16* src = Wo + (size_t)(512 * w + (lane >> 1)) * DM + 16 * c + 8 * (lane & 1);
  v4u tmp[16];
#pragma unroll
  for (int ks = 0; ks < 16; ++ks) tmp[ks] = *(const v4u*)(src + (size_t)ks * 32 * DM);
  char* base = smem + w * 16384;
#pragma unroll
  for (int ks = 0; ks < 16; ++ks) *(v4u*)(base + ks * 1024 + lane * 16) = tmp[ks];
  __syncthreads();
  const int g = lane >> 4, qq = (lane & 15) >> 2, p = lane & 3;
#pragma unroll
  for (int ks = 0; ks < 16; ++ks) {
    s16x4 lo = tr_read(base + ks * 1024 + (8 * g + qq) * 32 + p * 8);
    s16x4 hi = tr_read(base + ks * 1024 + (8 * g + 4 + qq) * 32 + p * 8);
    wb[ks] = cat4(lo, hi);
  }
  __syncthreads();
}

DI void role_s2(const Params& P, char* smem, int c, int hf) {
  const int t = threadIdx.x, lane = t & 63, w = t >> 6, q = lane >> 4, l15 = lane & 15;
  bf16x8 wb[16];
  load_wo16(P.Wo, c, smem, wb, t);
  char* ws = P.ws;
  int dead = 0;
  float* red = (float*)smem;
  for (int s = 0; s < NSEG - 1; ++s) {
    v4u a[4];
    const char* src = ws + OFF_OBUF + (size_t)s * 65536 + w * 8192 + (l15 >> 2) * 2048 + (q >> 1) * 256 + (4 * hf + (l15 & 3)) * 32 + (q & 1) * 16;
    const char* prb = ws + OFF_OBUF + (size_t)s * 65536 + w * 8192 + (c & 31) * 256 + hf * 128 + 124;
    if (S2_PRE > 0 && s > 0) { __builtin_amdgcn_s_sleep(S2_PRE / 2); __builtin_amdgcn_s_sleep(S2_PRE - S2_PRE / 2); }
    for (int sp = 0;; ++sp) {
      unsigned pv = ld4_sc1(prb);
      if (__builtin_amdgcn_readfirstlane(pv) != SENT) break;
      if (dead || sp > SPIN_CAP) { dead = 1; break; }
    }
    if (S2_POST > 0) __builtin_amdgcn_s_sleep(S2_POST);
    {
      bool ok = false;
      for (int sp = 0;; ++sp) {
        if (!ok) {
          asm volatile(
            "global_load_dwordx4 %0, %4, off sc1\n\t"
            "global_load_dwordx4 %1, %4, off offset:512 sc1\n\t"
            "global_load_dwordx4 %2, %4, off offset:1024 sc1\n\t"
            "global_load_dwordx4 %3, %4, off offset:1536 sc1\n\t"
            "s_waitcnt vmcnt(0)"
            : "=&v"(a[0]), "=&v"(a[1]), "=&v"(a[2]), "=&v"(a[3]) : "v"(src) : "memory");
          ok = valid4(a[0]) && valid4(a[1]) && valid4(a[2]) && valid4(a[3]);
        }
        if (__all(ok)) break;
        if (dead || sp > SPIN_CAP) { dead = 1; break; }
      }
    }
    STAMP(s, 0);
    f32x4 acc[4];
#pragma unroll
    for (int kq = 0; kq < 4; ++kq) acc[kq] = (f32x4){0.f, 0.f, 0.f, 0.f};
#pragma unroll
    for (int i = 0; i < 4; ++i)
#pragma unroll
      for (int kq = 0; kq < 4; ++kq) acc[kq] = mfma16(as_frag(a[i]), wb[4 * kq + i], acc[kq]);
    const f32x4 av = (q == 0) ? acc[0] : (q == 1) ? acc[1] : (q == 2) ? acc[2] : acc[3];
    float* rb = red + (s & 1) * 2048 + (w * 4 + q) * 64;
#pragma unroll
    for (int r = 0; r < 4; ++r) rb[r * 16 + l15] = av[r];
    __syncthreads();
    STAMP(s, 1);
    if (t < 256) {
      const int g = t >> 5, pi = t & 31;
      const float* rp = red + (s & 1) * 2048 + pi * 64 + g * 8;
      const f32x4 x0 = *(const f32x4*)rp, x1 = *(const f32x4*)(rp + 4);
      float y[8];
#pragma unroll
      for (int i = 0; i < 4; ++i) { y[i] = row_sum16(x0[i]); y[4 + i] = row_sum16(x1[i]); }
#pragma unroll
      for (int i = 0; i < 8; ++i) {
        unsigned u = __builtin_bit_cast(unsigned, y[i]);
        auto r = __builtin_amdgcn_permlane16_swap(u, u, false, false);
        y[i] = __builtin_bit_cast(float, (unsigned)r[0]) + __builtin_bit_cast(float, (unsigned)r[1]);
      }
      if (pi == 0) {
        v4u o = {unsent(pack2(y[0], y[1])), unsent(pack2(y[2], y[3])), unsent(pack2(y[4], y[5])), unsent(pack2(y[6], y[7]))};
        st16_sc1(ws + OFF_MEMBUF + (size_t)(s + 1) * 10240 + c * 256 + hf * 128 + g * 16, o);
      }
    }
    STAMP(s, 2);
  }
}

DI void role_s4(const Params& P, char* smem, int h, int par) {
  const int t = threadIdx.x, lane = t & 63, w = t >> 6, q = lane >> 4, l15 = lane & 15;
  char* ws = P.ws;
  int dead = 0;
  {
    bf16x8 wr[48];
    load_w3(P.Wq, P.Wk, P.Wv, h, smem, wr, t);
    v4u pa[3];
    auto fetchA = [&](int j) {
      const int row0 = (2 * (j >> 1) + par) * 32 + (j & 1) * 16;
      const char* src = (const char*)P.ax + (size_t)row0 * (DM * 2);
      pa[0] = *(const v4u*)(src + t * 16);
      pa[1] = *(const v4u*)(src + (512 + t) * 16);
      if (t < 256) pa[2] = *(const v4u*)(src + (1024 + t) * 16);
    };
    auto putA = [&](int buf) {
      char* dst = smem + S4_A + buf * 16 * AST;
      *(v4u*)(dst + (t / 80) * AST + (t % 80) * 16) = pa[0];
      *(v4u*)(dst + ((512 + t) / 80) * AST + ((512 + t) % 80) * 16) = pa[1];
      if (t < 256) *(v4u*)(dst + ((1024 + t) / 80) * AST + ((1024 + t) % 80) * 16) = pa[2];
    };
    fetchA(0); putA(0); fetchA(1);
    __syncthreads();
    for (int j = 0; j < 64; ++j) {
      const int seg = 2 * (j >> 1) + par, mt = j & 1;
      f32x4 aq, ak, av;
      proj3(smem + S4_A + (j & 1) * 16 * AST, l15, smem, wr, t, aq, ak, av);
      char* T = smem + S4_T + (j & 1) * 16 * TST;
#pragma unroll
      for (int r = 0; r < 4; ++r) {
        *(u16*)(T + (4 * q + r) * TST + (16 * w + l15) * 2) = f2bf(aq[r]);
        *(u16*)(T + (4 * q + r) * TST + 256 + (16 * w + l15) * 2) = f2bf(ak[r]);
      }
      {
        v2u vv = {pack2(av[0], av[1]), pack2(av[2], av[3])};
        st8_sc1(ws + OFF_VXT + ((size_t)(seg * NH + h)) * 8192 + (16 * w + l15) * 64 + (16 * mt + 4 * q) * 2, vv);
      }
      if (j + 1 < 64) { putA((j + 1) & 1); if (j + 2 < 64) fetchA(j + 2); }
      __syncthreads();
      {
        const int row = t >> 5, ch = t & 31;
        v4u v = *(const v4u*)(T + row * TST + ch * 16);
        const size_t o = ((size_t)(seg * NH + h)) * 8192 + (16 * mt + row) * 256 + (ch & 15) * 16;
        if (ch < 16) *(v4u*)(ws + OFF_QX + o) = v; else st16_sc1(ws + OFF_KX + o, v);
      }
      if (mt == 1) {
        drain();
        __syncthreads();
        if (t == 0) st4_sc1((unsigned*)(ws + OFF_FLAGKV) + h * 64 + seg, 1u);
        STAMP(j >> 1, 0);
      }
    }
  }
  const float scale = 0.08838834764831845f;
  const int qt = w & 1, dq = w >> 1;
  for (int i = 0; i < 32; ++i) {
    const int seg = 2 * i + par;
    wait_word((const unsigned*)(ws + OFF_FLAGM) + h * 64 + seg, 1u, dead, seg == NSEG - 1);
    const size_t sh = (size_t)(seg * NH + h);
    const char* km = ws + OFF_KM + sh * 2048;
    const char* vm = ws + OFF_VMT + sh * 2048;
    const char* kx = ws + OFF_KX + sh * 8192;
    const char* vx = ws + OFF_VXT + sh * 8192;
    const char* qx = ws + OFF_QX + sh * 8192;
    v4u kf[3][4], qf[4];
    {
      const char* p0 = (l15 < 8) ? (km + l15 * 256) : (kx + (l15 - 8) * 256);
      const char* p1 = kx + (8 + l15) * 256;
      const char* p2 = kx + (24 + (l15 & 7)) * 256;
      const char* pq = qx + (16 * qt + l15) * 256;
      v4u tmp[16];
      asm volatile(
        "global_load_dwordx4 %0, %16, off sc1\n\t"
        "global_load_dwordx4 %1, %16, off offset:64 sc1\n\t"
        "global_load_dwordx4 %2, %16, off offset:128 sc1\n\t"
        "global_load_dwordx4 %3, %16, off offset:192 sc1\n\t"
        "global_load_dwordx4 %4, %17, off sc1\n\t"
        "global_load_dwordx4 %5, %17, off offset:64 sc1\n\t"
        "global_load_dwordx4 %6, %17, off offset:128 sc1\n\t"
        "global_load_dwordx4 %7, %17, off offset:192 sc1\n\t"
        "global_load_dwordx4 %8, %18, off sc1\n\t"
        "global_load_dwordx4 %9, %18, off offset:64 sc1\n\t"
        "global_load_dwordx4 %10, %18, off offset:128 sc1\n\t"
        "global_load_dwordx4 %11, %18, off offset:192 sc1\n\t"
        "global_load_dwordx4 %12, %19, off sc1\n\t"
        "global_load_dwordx4 %13, %19, off offset:64 sc1\n\t"
        "global_load_dwordx4 %14, %19, off offset:128 sc1\n\t"
        "global_load_dwordx4 %15, %19, off offset:192 sc1\n\t"
        "s_waitcnt vmcnt(0)"
        : "=&v"(tmp[0]), "=&v"(tmp[1]), "=&v"(tmp[2]), "=&v"(tmp[3]), "=&v"(tmp[4]), "=&v"(tmp[5]), "=&v"(tmp[6]), "=&v"(tmp[7]),
          "=&v"(tmp[8]), "=&v"(tmp[9]), "=&v"(tmp[10]), "=&v"(tmp[11]), "=&v"(tmp[12]), "=&v"(tmp[13]), "=&v"(tmp[14]), "=&v"(tmp[15])
        : "v"(p0 + q * 16), "v"(p1 + q * 16), "v"(p2 + q * 16), "v"(pq + q * 16) : "memory");
#pragma unroll
      for (int ks = 0; ks < 4; ++ks) { kf[0][ks] = tmp[ks]; kf[1][ks] = tmp[4 + ks]; kf[2][ks] = tmp[8 + ks]; qf[ks] = tmp[12 + ks]; }
    }
    f32x4 st[3];
#pragma unroll
    for (int kt = 0; kt < 3; ++kt) {
      st[kt] = (f32x4){0.f, 0.f, 0.f, 0.f};
#pragma unroll
      for (int ks = 0; ks < 4; ++ks) st[kt] = mfma16(as_frag(kf[kt][ks]), as_frag(qf[ks]), st[kt]);
    }
    const int qi = 16 * qt + l15;
    bf16x8 pb0, pb1;
    softmax_pt(st, scale, [&](int kt, int r) { const int key = 16 * kt + 4 * q + r; return key < 8 || (key < 40 && key - 8 <= qi); }, pb0, pb1);
    unsigned ok[2][2];
#pragma unroll
    for (int dd = 0; dd < 2; ++dd) {
      const int d = 16 * (2 * dq + dd) + l15;
      const char* pa = (q < 2) ? (vm + d * 16 + q * 8) : (vx + d * 64 + (4 * q - 8) * 2);
      const char* pb = vx + d * 64 + (8 + 4 * q) * 2;
      const char* pc = vx + d * 64 + (24 + 4 * (q & 1)) * 2;
      v2u a, b, c;
      asm volatile("global_load_dwordx2 %0, %3, off sc1\n\tglobal_load_dwordx2 %1, %4, off sc1\n\tglobal_load_dwordx2 %2, %5, off sc1\n\ts_waitcnt vmcnt(0)"
                   : "=&v"(a), "=&v"(b), "=&v"(c) : "v"(pa), "v"(pb), "v"(pc) : "memory");
      if (q >= 2) c = (v2u){0, 0};
      v4u f0 = {a.x, a.y, b.x, b.y}, f1 = {c.x, c.y, 0, 0};
      f32x4 o = {0.f, 0.f, 0.f, 0.f};
      o = mfma16(as_frag(f0), pb0, o);
      o = mfma16(as_frag(f1), pb1, o);
      ok[dd][0] = pack2(o[0], o[1]); ok[dd][1] = pack2(o[2], o[3]);
    }
    {
      auto r0 = __builtin_amdgcn_permlane16_swap(ok[0][0], ok[0][0], false, false);
      auto r1 = __builtin_amdgcn_permlane16_swap(ok[0][1], ok[0][1], false, false);
      auto r2 = __builtin_amdgcn_permlane16_swap(ok[1][0], ok[1][0], false, false);
      auto r3 = __builtin_amdgcn_permlane16_swap(ok[1][1], ok[1][1], false, false);
      char* dst = ws + OFF_OX + ((size_t)((seg * 2 + qt) * NH + h)) * 4096 + (2 * dq + (q & 1)) * 512 + l15 * 32 + (q >> 1) * 16;
      v4u ov = (q & 1) ? (v4u){(unsigned)r2[0], (unsigned)r3[0], (unsigned)r2[1], (unsigned)r3[1]}
                       : (v4u){(unsigned)r0[0], (unsigned)r1[0], (unsigned)r0[1], (unsigned)r1[1]};
      st16_sc1(dst, ov);
    }
    drain();
    __syncthreads();
    if (t == 0) __hip_atomic_fetch_add((unsigned*)(ws + OFF_CNTOX + seg * 64), 1u, __ATOMIC_RELAXED, __HIP_MEMORY_SCOPE_AGENT);
    STAMP(i, 1);
  }
}

DI void role_s3(const Params& P, char* smem, int c, int par) {
  const int t = threadIdx.x, lane = t & 63, w = t >> 6, q = lane >> 4, l15 = lane & 15;
  bf16x8 wb0[16], wb1[16];
  load_wo16(P.Wo, 2 * c, smem, wb0, t);
  load_wo16(P.Wo, 2 * c + 1, smem, wb1, t);
  char* ws = P.ws;
  int dead = 0;
  float* red = (float*)smem;
  for (int i = 0; i < 32; ++i) {
    const int seg = 2 * i + par;
    wait_word((const unsigned*)(ws + OFF_CNTOX + seg * 64), 31u, dead, seg == NSEG - 1);
    float* rb = red + (i & 1) * 8192 + w * 1024;
#pragma unroll
    for (int mt = 0; mt < 2; ++mt) {
      f32x4 acc0 = {0.f, 0.f, 0.f, 0.f}, acc1 = acc0;
#pragma unroll
      for (int hf = 0; hf < 2; ++hf) {
        v4u a[8];
        ld16x8s1k_sc1(ws + OFF_OX + ((size_t)((seg * 2 + mt) * NH + 4 * w)) * 4096 + hf * 8192 + (q >> 1) * 512 + l15 * 32 + (q & 1) * 16, a);
#pragma unroll
        for (int ks = 0; ks < 8; ++ks) {
          acc0 = mfma16(as_frag(a[ks]), wb0[hf * 8 + ks], acc0);
          acc1 = mfma16(as_frag(a[ks]), wb1[hf * 8 + ks], acc1);
        }
        if (seg < NSEG - 1) __builtin_amdgcn_s_sleep(S3_SLEEP);
      }
#pragma unroll
      for (int r = 0; r < 4; ++r) { rb[(16 * mt + 4 * q + r) * 32 + l15] = acc0[r]; rb[(16 * mt + 4 * q + r) * 32 + 16 + l15] = acc1[r]; }
    }
    __syncthreads();
    {
      const int row = t >> 4, cp = t & 15;
      const float* rp = red + (i & 1) * 8192 + row * 32 + 2 * cp;
      float x0 = 0.f, x1 = 0.f;
#pragma unroll
      for (int ww = 0; ww < 8; ++ww) { x0 += rp[ww * 1024]; x1 += rp[ww * 1024 + 1]; }
      *(unsigned*)(P.out + (size_t)(seg * 32 + row) * OUTW + 32 * c + 2 * cp) = pack2(x0, x1);
    }
    STAMP(i, 0);
  }
}

constexpr int PB_A = 0;
constexpr int PB_R = 2 * 16 * AST;
constexpr int RST = 496;
constexpr int PB_O = PB_R + 3 * 16 * RST;
constexpr int OST = 176;
constexpr int PB_STG = 81920;
DI void role_pb(const Params& P, char* smem, int g, int rb) {
  const int t = threadIdx.x, lane = t & 63, w = t >> 6, q = lane >> 4, l15 = lane & 15;
  char* ws = P.ws;
  int dead = 0;
  const int gg = lane >> 4, qq = (lane & 15) >> 2, pp = lane & 3;
  {
    bf16x8 wb[40];
#pragma unroll
    for (int nn = 0; nn < 2; ++nn) {
      const int n = (2 * w + nn) < 15 ? (2 * w + nn) : 14;
      const int col0 = (n / 5) * DM + 80 * g + 16 * (n % 5);
      const u16* src = P.wqkv + (size_t)(lane >> 1) * 1920 + col0 + 8 * (lane & 1);
#pragma unroll
      for (int half = 0; half < 2; ++half) {
        v4u tmp[10];
#pragma unroll
        for (int i = 0; i < 10; ++i) tmp[i] = *(const v4u*)(src + (size_t)(half * 10 + i) * 32 * 1920);
        char* base = smem + PB_STG + w * 10240;
#pragma unroll
        for (int i = 0; i < 10; ++i) *(v4u*)(base + i * 1024 + lane * 16) = tmp[i];
        __syncthreads();
#pragma unroll
        for (int i = 0; i < 10; ++i) {
          s16x4 lo = tr_read(base + i * 1024 + (8 * gg + qq) * 32 + pp * 8);
          s16x4 hi = tr_read(base + i * 1024 + (8 * gg + 4 + qq) * 32 + pp * 8);
          wb[nn * 20 + half * 10 + i] = cat4(lo, hi);
        }
        __syncthreads();
      }
    }
    v4u pa[3];
    auto fetchA = [&](int j) {
      const int row0 = 512 * rb - 16 + 16 * j;
      if (row0 < 0) { pa[0] = (v4u){0, 0, 0, 0}; pa[1] = pa[0]; pa[2] = pa[0]; return; }
      const char* src = (const char*)P.bx + (size_t)row0 * (DM * 2);
      pa[0] = *(const v4u*)(src + t * 16);
      pa[1] = *(const v4u*)(src + (512 + t) * 16);
      if (t < 256) pa[2] = *(const v4u*)(src + (1024 + t) * 16);
    };
    auto putA = [&](int buf) {
      char* dst = smem + PB_A + buf * 16 * AST;
      *(v4u*)(dst + (t / 80) * AST + (t % 80) * 16) = pa[0];
      *(v4u*)(dst + ((512 + t) / 80) * AST + ((512 + t) % 80) * 16) = pa[1];
      if (t < 256) *(v4u*)(dst + ((1024 + t) / 80) * AST + ((1024 + t) % 80) * 16) = pa[2];
    };
    fetchA(0); putA(0); fetchA(1);
    __syncthreads();
    const float scale = 0.22360679774997896f;
    for (int j = 0; j < 33; ++j) {
      const char* ap = smem + PB_A + (j & 1) * 16 * AST + l15 * AST + q * 16;
      f32x4 c0 = {0.f, 0.f, 0.f, 0.f}, c1 = c0;
#pragma unroll
      for (int ks = 0; ks < 20; ++ks) {
        bf16x8 a = lds_frag(ap + ks * 64);
        c0 = mfma16(a, wb[ks], c0);
        c1 = mfma16(a, wb[20 + ks], c1);
      }
      char* R = smem + PB_R + (j % 3) * 16 * RST;
#pragma unroll
      for (int r = 0; r < 4; ++r) {
        *(u16*)(R + (4 * q + r) * RST + (32 * w + l15) * 2) = f2bf(c0[r]);
        if (w < 7) *(u16*)(R + (4 * q + r) * RST + (32 * w + 16 + l15) * 2) = f2bf(c1[r]);
      }
      if (j + 1 < 33) { putA((j + 1) & 1); if (j + 2 < 33) fetchA(j + 2); }
      __syncthreads();
      if (j >= 2 && t < 160) {
        const int row = t / 10, ch = t % 10;
        v4u v = *(const v4u*)(smem + PB_O + ((j - 1) & 1) * 16 * OST + row * OST + ch * 16);
        st16_sc1(ws + OFF_OB + (size_t)(512 * rb - 16 + 16 * (j - 1) + row) * 1280 + (80 * g) * 2 + ch * 16, v);
      }
      if (j >= 1) {
        const int item = t >> 3, wi = t & 7, row = item >> 2, hh = item & 3;
        const int srow = 512 * rb - 16 + 16 * j + row;
        const int krow = row - 2 * wi;
        const bool vis = (srow - 2 * wi) >= 0;
        const char* Rc = smem + PB_R + (j % 3) * 16 * RST;
        const char* Rp = smem + PB_R + ((j + 2) % 3) * 16 * RST;
        const char* qrow = Rc + row * RST + (20 * hh) * 2;
        const char* krowp = (krow >= 0 ? Rc + krow * RST : Rp + (krow + 16) * RST);
        float dot = 0.f;
#pragma unroll
        for (int d = 0; d < 20; d += 2) {
          unsigned qv = *(const unsigned*)(qrow + d * 2);
          unsigned kv = *(const unsigned*)(krowp + (80 + 20 * hh + d) * 2);
          dot += bf2f((u16)qv) * bf2f((u16)kv);
          dot += bf2f((u16)(qv >> 16)) * bf2f((u16)(kv >> 16));
        }
        float lg = rbf(rbf(dot) * scale);
        float mx = vis ? lg : -3.0e38f;
        mx = fmaxf(mx, __shfl_xor(mx, 1)); mx = fmaxf(mx, __shfl_xor(mx, 2)); mx = fmaxf(mx, __shfl_xor(mx, 4));
        float e = vis ? __expf(lg - mx) : 0.f;
        float sum = e;
        sum += __shfl_xor(sum, 1); sum += __shfl_xor(sum, 2); sum += __shfl_xor(sum, 4);
        const float pw = rbf(e * (1.0f / sum));
        float o0 = 0.f, o1 = 0.f, o2 = 0.f;
#pragma unroll
        for (int ww = 0; ww < 8; ++ww) {
          const float pv = __shfl(pw, (lane & ~7) + ww);
          const int kr = row - 2 * ww;
          const char* vr = (kr >= 0 ? Rc + kr * RST : Rp + (kr + 16) * RST) + (160 + 20 * hh) * 2;
          o0 += pv * bf2f(*(const u16*)(vr + wi * 2));
          o1 += pv * bf2f(*(const u16*)(vr + (wi + 8) * 2));
          if (wi < 4) o2 += pv * bf2f(*(const u16*)(vr + (wi + 16) * 2));
        }
        char* O = smem + PB_O + (j & 1) * 16 * OST + row * OST + (20 * hh) * 2;
        *(u16*)(O + wi * 2) = f2bf(o0);
        *(u16*)(O + (wi + 8) * 2) = f2bf(o1);
        if (wi < 4) *(u16*)(O + (wi + 16) * 2) = f2bf(o2);
      }
    }
    __syncthreads();
    if (t < 160) {
      const int row = t / 10, ch = t % 10;
      v4u v = *(const v4u*)(smem + PB_O + (32 & 1) * 16 * OST + row * OST + ch * 16);
      st16_sc1(ws + OFF_OB + (size_t)(512 * rb - 16 + 16 * 32 + row) * 1280 + (80 * g) * 2 + ch * 16, v);
    }
    drain();
    __syncthreads();
    if (t == 0) __hip_atomic_fetch_add((unsigned*)(ws + OFF_CNTB + rb * 64), 1u, __ATOMIC_RELAXED, __HIP_MEMORY_SCOPE_AGENT);
  }
  {
    char* stg = smem + 102400 + w * 7168;
    for (int f0 = w * 13; f0 < w * 13 + 13 && f0 < 100; f0 += 7) {
      v4u tmp[7];
#pragma unroll
      for (int i = 0; i < 7; ++i) {
        const int f = f0 + i; const bool okf = (f < w * 13 + 13) && (f < 100);
        const int ff = okf ? f : 0;
        tmp[i] = *(const v4u*)(P.wo + (size_t)((ff % 20) * 32 + (lane >> 1)) * DM + 80 * g + 16 * (ff / 20) + 8 * (lane & 1));
      }
#pragma unroll
      for (int i = 0; i < 7; ++i) *(v4u*)(stg + i * 1024 + lane * 16) = tmp[i];
      __builtin_amdgcn_s_waitcnt(0xC07F);
#pragma unroll
      for (int i = 0; i < 7; ++i) {
        const int f = f0 + i; const bool okf = (f < w * 13 + 13) && (f < 100);
        s16x4 lo = tr_read(stg + i * 1024 + (8 * gg + qq) * 32 + pp * 8);
        s16x4 hi = tr_read(stg + i * 1024 + (8 * gg + 4 + qq) * 32 + pp * 8);
        if (okf) *(bf16x8*)(smem + f * 1024 + lane * 16) = cat4(lo, hi);
      }
      __builtin_amdgcn_s_waitcnt(0xC07F);
    }
    __syncthreads();
    wait_word((const unsigned*)(ws + OFF_CNTB + rb * 64), 7u, dead);
    for (int i = 0; i < 4; ++i) {
      const int row0 = 512 * rb + 16 * (w + 8 * i);
      v4u a0[10], a1[10];
      ld16x10_sc1(ws + OFF_OB + (size_t)(row0 + l15) * 1280 + q * 16, a0);
      ld16x10_sc1(ws + OFF_OB + (size_t)(row0 + l15) * 1280 + 640 + q * 16, a1);
#pragma unroll
      for (int n = 0; n < 5; ++n) {
        f32x4 acc = {0.f, 0.f, 0.f, 0.f};
#pragma unroll
        for (int ks = 0; ks < 10; ++ks) acc = mfma16(as_frag(a0[ks]), lds_frag(smem + (n * 20 + ks) * 1024 + lane * 16), acc);
#pragma unroll
        for (int ks = 0; ks < 10; ++ks) acc = mfma16(as_frag(a1[ks]), lds_frag(smem + (n * 20 + 10 + ks) * 1024 + lane * 16), acc);
#pragma unroll
        for (int r = 0; r < 4; ++r) P.out[(size_t)(row0 + 4 * q + r) * OUTW + DM + 80 * g + 16 * n + l15] = f2bf(acc[r]);
      }
    }
  }
}

__global__ __launch_bounds__(512, 2) void fused_kernel(Params P) {
  extern __shared__ __attribute__((aligned(16))) char smem[];
  const int b = blockIdx.x;
  STAMP(63, 7);
  if (b < 32) role_s1(P, smem, b);
  else if (b < 112) role_s2(P, smem, (b - 32) % 40, (b - 32) / 40);
  else if (b < 176) role_s4(P, smem, (b - 112) & 31, (b - 112) >> 5);
  else if (b < 216) role_s3(P, smem, (b - 176) % 20, (b - 176) / 20);
  else if (b < 248) role_pb(P, smem, (b - 216) & 7, (b - 216) >> 3);
  STAMP(63, 6);
}

extern "C" void kernel_launch(void* const* d_in, const int* in_sizes, int n_in,
                              void* d_out, int out_size, void* d_ws, size_t ws_size,
                              hipStream_t stream) {
  Params P;
  P.ax = (const u16*)d_in[0]; P.mem0 = (const u16*)d_in[1];
  P.Wq = (const u16*)d_in[2]; P.Wk = (const u16*)d_in[3]; P.Wv = (const u16*)d_in[4]; P.Wo = (const u16*)d_in[5];
  P.bx = (const u16*)d_in[7]; P.wqkv = (const u16*)d_in[8]; P.wo = (const u16*)d_in[9];
  P.out = (u16*)d_out; P.ws = (char*)d_ws;
  hipFuncSetAttribute((const void*)fused_kernel, hipFuncAttributeMaxDynamicSharedMemorySize, LDS_BYTES);
  hipMemsetAsync(d_ws, 0xFF, POISON_BYTES, stream);
  fused_kernel<<<256, 512, LDS_BYTES, stream>>>(P);
}
```

```cpp
#include <hip/hip_runtime.h>
#include <hip/hip_bf16.h>
#include <stdint.h>

typedef unsigned short u16;
typedef short bf16x8 __attribute__((ext_vector_type(8)));
typedef short s16x4 __attribute__((ext_vector_type(4)));
typedef float f32x4 __attribute__((ext_vector_type(4)));
typedef unsigned v4u __attribute__((ext_vector_type(4)));
typedef unsigned v2u __attribute__((ext_vector_type(2)));
#define LAS __attribute__((address_space(3)))
#define DI __device__ __forceinline__

constexpr int SLEN = 2048, DM = 640, NH = 32, DH = 128, HD = 4096, NSEG = 64;
constexpr int OUTW = 1280;
constexpr size_t OFF_FLAGKV = 0;
constexpr size_t OFF_FLAGM  = 8192;
constexpr size_t OFF_CNTOX  = 16384;
constexpr size_t OFF_CNTB   = 20480;
constexpr size_t OFF_MEMBUF = 24576;
constexpr size_t OFF_OBUF   = OFF_MEMBUF + 64 * 10240;
constexpr size_t POISON_BYTES = OFF_OBUF + (size_t)64 * 65536;
constexpr size_t OFF_KX  = (size_t)8 << 20;
constexpr size_t OFF_VXT = (size_t)24 << 20;
constexpr size_t OFF_QX  = (size_t)40 << 20;
constexpr size_t OFF_KM  = (size_t)56 << 20;
constexpr size_t OFF_VMT = (size_t)60 << 20;
constexpr size_t OFF_OX  = (size_t)64 << 20;
constexpr size_t OFF_OB  = (size_t)80 << 20;
constexpr unsigned SENT = 0xFFFFFFFFu;
constexpr int SPIN_CAP = 300000;
constexpr int LDS_BYTES = 160 * 1024;
#define POLL_SLEEP 60
#define S1_PRE 28
#define S2_PRE 76
#define S1_POST 0
#define S2_POST 0
#define S3_SLEEP 8

DI float bf2f(u16 b) { return __builtin_bit_cast(float, (unsigned)b << 16); }
DI u16 f2bf(float f) { __bf16 b = (__bf16)f; return __builtin_bit_cast(u16, b); }
DI float rbf(float f) { return bf2f(f2bf(f)); }
DI unsigned pack2(float a, float b) { return (unsigned)f2bf(a) | ((unsigned)f2bf(b) << 16); }
DI unsigned unsent(unsigned x) { return x == SENT ? 0xFFFEFFFFu : x; }
DI bool valid4(v4u v) { return v.x != SENT && v.y != SENT && v.z != SENT && v.w != SENT; }
DI f32x4 mfma16(bf16x8 a, bf16x8 b, f32x4 c) { return __builtin_amdgcn_mfma_f32_16x16x32_bf16(a, b, c, 0, 0, 0); }
DI bf16x8 as_frag(v4u v) { return __builtin_bit_cast(bf16x8, v); }
DI s16x4 tr_read(const char* p) {
  typedef short v4i16_t __attribute__((ext_vector_type(4)));
  return __builtin_amdgcn_ds_read_tr16_b64_v4i16((LAS v4i16_t*)(p));
}
DI float red_max(float x) {
  unsigned u = __builtin_bit_cast(unsigned, x);
  auto r = __builtin_amdgcn_permlane16_swap(u, u, false, false);
  x = fmaxf(__builtin_bit_cast(float, (unsigned)r[0]), __builtin_bit_cast(float, (unsigned)r[1]));
  u = __builtin_bit_cast(unsigned, x);
  auto r2 = __builtin_amdgcn_permlane32_swap(u, u, false, false);
  return fmaxf(__builtin_bit_cast(float, (unsigned)r2[0]), __builtin_bit_cast(float, (unsigned)r2[1]));
}
DI float red_sum(float x) {
  unsigned u = __builtin_bit_cast(unsigned, x);
  auto r = __builtin_amdgcn_permlane16_swap(u, u, false, false);
  x = __builtin_bit_cast(float, (unsigned)r[0]) + __builtin_bit_cast(float, (unsigned)r[1]);
  u = __builtin_bit_cast(unsigned, x);
  auto r2 = __builtin_amdgcn_permlane32_swap(u, u, false, false);
  return __builtin_bit_cast(float, (unsigned)r2[0]) + __builtin_bit_cast(float, (unsigned)r2[1]);
}
DI bf16x8 cat4(s16x4 lo, s16x4 hi) { bf16x8 r; r[0]=lo[0]; r[1]=lo[1]; r[2]=lo[2]; r[3]=lo[3]; r[4]=hi[0]; r[5]=hi[1]; r[6]=hi[2]; r[7]=hi[3]; return r; }
DI bf16x8 lds_frag(const char* p) { return *(const bf16x8*)p; }

DI v4u ld16_sc1(const void* p) { v4u r; asm volatile("global_load_dwordx4 %0, %1, off sc1\n\ts_waitcnt vmcnt(0)" : "=&v"(r) : "v"(p) : "memory"); return r; }
DI void ld16x2_sc1(const void* p0, const void* p1, v4u& a, v4u& b) {
  asm volatile("global_load_dwordx4 %0, %2, off sc1\n\tglobal_load_dwordx4 %1, %3, off sc1\n\ts_waitcnt vmcnt(0)"
               : "=&v"(a), "=&v"(b) : "v"(p0), "v"(p1) : "memory");
}
DI unsigned ld4_sc1(const void* p) { unsigned r; asm volatile("global_load_dword %0, %1, off sc1\n\ts_waitcnt vmcnt(0)" : "=&v"(r) : "v"(p) : "memory"); return r; }
DI void st16_sc1(void* p, v4u v) { asm volatile("global_store_dwordx4 %0, %1, off sc1\n\ts_nop 1" :: "v"(p), "v"(v) : "memory"); }
DI void st8_sc1(void* p, v2u v) { asm volatile("global_store_dwordx2 %0, %1, off sc1\n\ts_nop 1" :: "v"(p), "v"(v) : "memory"); }
DI void st4_sc1(void* p, unsigned v) { asm volatile("global_store_dword %0, %1, off sc1\n\ts_nop 1" :: "v"(p), "v"(v) : "memory"); }
DI float dpp_xor1(float x) { return __builtin_bit_cast(float, __builtin_amdgcn_update_dpp(0, __builtin_bit_cast(int, x), 0xB1, 0xf, 0xf, true)); }
DI float dpp_xor2(float x) { return __builtin_bit_cast(float, __builtin_amdgcn_update_dpp(0, __builtin_bit_cast(int, x), 0x4E, 0xf, 0xf, true)); }
DI float dpp_shl4(float x) { return __builtin_bit_cast(float, __builtin_amdgcn_update_dpp(0, __builtin_bit_cast(int, x), 0x104, 0xf, 0xf, true)); }
template <int CTRL> DI float dpp_ror(float x) { return __builtin_bit_cast(float, __builtin_amdgcn_update_dpp(0, __builtin_bit_cast(int, x), CTRL, 0xf, 0xf, true)); }
DI float row_sum16(float x) { x += dpp_ror<0x128>(x); x += dpp_ror<0x124>(x); x += dpp_ror<0x122>(x); x += dpp_ror<0x121>(x); return x; }
DI void drain() { asm volatile("s_waitcnt vmcnt(0)" ::: "memory"); }
DI void ld16x8s1k_sc1(const char* p, v4u (&a)[8]) {
  asm volatile(
    "global_load_dwordx4 %0, %8, off sc1\n\t"
    "global_load_dwordx4 %1, %8, off offset:1024 sc1\n\t"
    "global_load_dwordx4 %2, %8, off offset:2048 sc1\n\t"
    "global_load_dwordx4 %3, %8, off offset:3072 sc1\n\t"
    "global_load_dwordx4 %4, %9, off sc1\n\t"
    "global_load_dwordx4 %5, %9, off offset:1024 sc1\n\t"
    "global_load_dwordx4 %6, %9, off offset:2048 sc1\n\t"
    "global_load_dwordx4 %7, %9, off offset:3072 sc1\n\t"
    "s_waitcnt vmcnt(0)"
    : "=&v"(a[0]), "=&v"(a[1]), "=&v"(a[2]), "=&v"(a[3]), "=&v"(a[4]), "=&v"(a[5]), "=&v"(a[6]), "=&v"(a[7])
    : "v"(p), "v"(p + 4096) : "memory");
}
DI void ld16x8s512_sc1(const char* p, v4u (&a)[8]) {
  asm volatile(
    "global_load_dwordx4 %0, %8, off sc1\n\t"
    "global_load_dwordx4 %1, %8, off offset:512 sc1\n\t"
    "global_load_dwordx4 %2, %8, off offset:1024 sc1\n\t"
    "global_load_dwordx4 %3, %8, off offset:1536 sc1\n\t"
    "global_load_dwordx4 %4, %8, off offset:2048 sc1\n\t"
    "global_load_dwordx4 %5, %8, off offset:2560 sc1\n\t"
    "global_load_dwordx4 %6, %8, off offset:3072 sc1\n\t"
    "global_load_dwordx4 %7, %8, off offset:3584 sc1\n\t"
    "s_waitcnt vmcnt(0)"
    : "=&v"(a[0]), "=&v"(a[1]), "=&v"(a[2]), "=&v"(a[3]), "=&v"(a[4]), "=&v"(a[5]), "=&v"(a[6]), "=&v"(a[7])
    : "v"(p) : "memory");
}
DI void ld16x10_sc1(const char* p, v4u (&a)[10]) {
  asm volatile(
    "global_load_dwordx4 %0, %10, off sc1\n\t"
    "global_load_dwordx4 %1, %10, off offset:64 sc1\n\t"
    "global_load_dwordx4 %2, %10, off offset:128 sc1\n\t"
    "global_load_dwordx4 %3, %10, off offset:192 sc1\n\t"
    "global_load_dwordx4 %4, %10, off offset:256 sc1\n\t"
    "global_load_dwordx4 %5, %10, off offset:320 sc1\n\t"
    "global_load_dwordx4 %6, %10, off offset:384 sc1\n\t"
    "global_load_dwordx4 %7, %10, off offset:448 sc1\n\t"
    "global_load_dwordx4 %8, %10, off offset:512 sc1\n\t"
    "global_load_dwordx4 %9, %10, off offset:576 sc1\n\t"
    "s_waitcnt vmcnt(0)"
    : "=&v"(a[0]), "=&v"(a[1]), "=&v"(a[2]), "=&v"(a[3]), "=&v"(a[4]), "=&v"(a[5]), "=&v"(a[6]), "=&v"(a[7]),
      "=&v"(a[8]), "=&v"(a[9])
    : "v"(p) : "memory");
}
DI void wait_word(const unsigned* p, unsigned want, int& dead, bool fast = false) {
  if ((threadIdx.x >> 6) == 0 && !dead) {
    int sp = 0;
    for (; sp < SPIN_CAP; ++sp) {
      unsigned v = ld4_sc1(p);
      if (__builtin_amdgcn_readfirstlane(v) == want) break;
      if (fast) __builtin_amdgcn_s_sleep(4); else __builtin_amdgcn_s_sleep(POLL_SLEEP);
    }
    if (sp >= SPIN_CAP) dead = 1;
  }
  __syncthreads();
}

constexpr int AST = 1296;
constexpr int KST = 272;
constexpr int VST = 136;
constexpr int L_WRES = 0;
constexpr int L_STG = 98304;
constexpr int STG_BUF = 3 * 32 * KST;
constexpr int S1_A = 98304;
constexpr int S1_K = S1_A + 10496;
constexpr int S1_VT = S1_K + 48 * KST;
constexpr int S1_Q = S1_VT + 128 * VST;
constexpr int S4_A = 98304;
constexpr int S4_T = S4_A + 2 * 16 * AST;
constexpr int TST = 528;

DI void load_w3(const u16* __restrict__ Wq, const u16* __restrict__ Wk, const u16* __restrict__ Wv, int h,
                char* smem, bf16x8 (&wr)[48], int t) {
  const int lane = t & 63, w = t >> 6;
  const int r = t >> 4, c16 = t & 15;
  const size_t goff = (size_t)r * HD + h * DH + c16 * 8;
  const int g = lane >> 4, qq = (lane & 15) >> 2, p = lane & 3;
  const int troff = (8 * g + qq) * KST + (16 * w + 4 * p) * 2;
  constexpr int PD = 5;
  v4u pf[PD][3];
#pragma unroll
  for (int i = 0; i < PD; ++i) {
    pf[i][0] = *(const v4u*)(Wq + goff + (size_t)i * 32 * HD);
    pf[i][1] = *(const v4u*)(Wk + goff + (size_t)i * 32 * HD);
    pf[i][2] = *(const v4u*)(Wv + goff + (size_t)i * 32 * HD);
  }
#pragma unroll
  for (int ks = 0; ks < 20; ++ks) {
    char* stg = smem + L_STG + (ks & 1) * STG_BUF;
#pragma unroll
    for (int m = 0; m < 3; ++m) *(v4u*)(stg + m * 32 * KST + r * KST + c16 * 16) = pf[ks % PD][m];
    if (ks + PD < 20) {
      pf[ks % PD][0] = *(const v4u*)(Wq + goff + (size_t)(ks + PD) * 32 * HD);
      pf[ks % PD][1] = *(const v4u*)(Wk + goff + (size_t)(ks + PD) * 32 * HD);
      pf[ks % PD][2] = *(const v4u*)(Wv + goff + (size_t)(ks + PD) * 32 * HD);
    }
    __syncthreads();
#pragma unroll
    for (int m = 0; m < 3; ++m) {
      s16x4 lo = tr_read(stg + m * 32 * KST + troff);
      s16x4 hi = tr_read(stg + m * 32 * KST + troff + 4 * KST);
      bf16x8 f = cat4(lo, hi);
      if (ks < 16) wr[ks * 3 + m] = f;
      else *(bf16x8*)(smem + L_WRES + w * 12288 + ((ks - 16) * 3 + m) * 1024 + lane * 16) = f;
    }
  }
  __syncthreads();
}

DI void proj3(const char* ldsA, int arow, const char* smem, const bf16x8 (&wr)[48], int t, f32x4& aq, f32x4& ak, f32x4& av) {
  const int lane = t & 63, w = t >> 6, q = lane >> 4;
  const char* ap = ldsA + arow * AST + q * 16;
  int woff = w * 12288 + lane * 16;
  asm volatile("" : "+v"(woff));
  const char* wp = smem + L_WRES + woff;
  aq = (f32x4){0.f, 0.f, 0.f, 0.f}; ak = aq; av = aq;
#pragma unroll
  for (int ks = 0; ks < 20; ++ks) {
    bf16x8 a = lds_frag(ap + ks * 64);
    bf16x8 b0, b1, b2;
    if (ks < 16) { b0 = wr[ks * 3]; b1 = wr[ks * 3 + 1]; b2 = wr[ks * 3 + 2]; }
    else { b0 = lds_frag(wp + ((ks - 16) * 3) * 1024); b1 = lds_frag(wp + ((ks - 16) * 3 + 1) * 1024); b2 = lds_frag(wp + ((ks - 16) * 3 + 2) * 1024); }
    aq = mfma16(a, b0, aq);
    ak = mfma16(a, b1, ak);
    av = mfma16(a, b2, av);
    if ((ks & 3) == 3) { asm volatile("" : "+v"(aq), "+v"(ak), "+v"(av)); __builtin_amdgcn_sched_barrier(0); }
  }
}

DI void proj3_pipe(const char* ldsA, int arow, const char* smem, const bf16x8 (&wr)[48], int t, f32x4& aq, f32x4& ak, f32x4& av) {
  const int lane = t & 63, w = t >> 6, q = lane >> 4;
  const char* ap = ldsA + arow * AST + q * 16;
  int woff = w * 12288 + lane * 16;
  asm volatile("" : "+v"(woff));
  const char* wp = smem + L_WRES + woff;
  aq = (f32x4){0.f, 0.f, 0.f, 0.f}; ak = aq; av = aq;
  bf16x8 a[2][4];
#pragma unroll
  for (int i = 0; i < 4; ++i) a[0][i] = lds_frag(ap + ((i < 3) ? i : 16) * 64);
#pragma unroll
  for (int g = 0; g < 5; ++g) {
    bf16x8 b0, b1, b2;
    if (g < 4) { b0 = lds_frag(wp + (g * 3) * 1024); b1 = lds_frag(wp + (g * 3 + 1) * 1024); b2 = lds_frag(wp + (g * 3 + 2) * 1024); }
    if (g < 4) {
#pragma unroll
      for (int i = 0; i < 4; ++i) {
        const int ks = (g + 1 < 4) ? ((i < 3) ? 3 * (g + 1) + i : 16 + g + 1) : 12 + i;
        a[(g + 1) & 1][i] = lds_frag(ap + ks * 64);
      }
    }
#pragma unroll
    for (int i = 0; i < 4; ++i) {
      if (g < 4 && i == 3) {
        aq = mfma16(a[g & 1][i], b0, aq); ak = mfma16(a[g & 1][i], b1, ak); av = mfma16(a[g & 1][i], b2, av);
      } else {
        const int ks = (g < 4) ? 3 * g + i : 12 + i;
        aq = mfma16(a[g & 1][i], wr[ks * 3], aq); ak = mfma16(a[g & 1][i], wr[ks * 3 + 1], ak); av = mfma16(a[g & 1][i], wr[ks * 3 + 2], av);
      }
    }
    asm volatile("" : "+v"(aq), "+v"(ak), "+v"(av)); __builtin_amdgcn_sched_barrier(0);
  }
}

template <class VIS>
DI void softmax_pt(const f32x4 (&st)[3], float scale, VIS vis, bf16x8& pb0, bf16x8& pb1) {
  float s[3][4]; float mx = -3.0e38f;
#pragma unroll
  for (int kt = 0; kt < 3; ++kt)
#pragma unroll
    for (int r = 0; r < 4; ++r) {
      float v = rbf(rbf(st[kt][r]) * scale);
      s[kt][r] = v;
      if (vis(kt, r)) mx = fmaxf(mx, v);
    }
  mx = red_max(mx);
  float sum = 0.f;
#pragma unroll
  for (int kt = 0; kt < 3; ++kt)
#pragma unroll
    for (int r = 0; r < 4; ++r) {
      float e = vis(kt, r) ? __expf(s[kt][r] - mx) : 0.f;
      s[kt][r] = e; sum += e;
    }
  sum = red_sum(sum);
  const float inv = 1.0f / sum;
#pragma unroll
  for (int r = 0; r < 4; ++r) {
    pb0[r] = (short)f2bf(s[0][r] * inv);
    pb0[4 + r] = (short)f2bf(s[1][r] * inv);
    pb1[r] = (short)f2bf(s[2][r] * inv);
    pb1[4 + r] = 0;
  }
}

DI void softmax_mem(const f32x4 (&st)[3], float scale, int q, int hi, bf16x8& pb0, bf16x8& pb1) {
  float s[6];
#pragma unroll
  for (int r = 0; r < 4; ++r) s[r] = hi ? st[1][r] : st[0][r];
  s[4] = hi ? st[2][2] : st[2][0];
  s[5] = hi ? st[2][3] : st[2][1];
  const bool v2 = q < 2;
  float mx = -3.0e38f;
#pragma unroll
  for (int i = 0; i < 6; ++i) { s[i] = rbf(rbf(s[i]) * scale); if (i < 4 || v2) mx = fmaxf(mx, s[i]); }
  mx = fmaxf(mx, dpp_ror<0x128>(mx));
  mx = red_max(mx);
  float sum = 0.f;
#pragma unroll
  for (int i = 0; i < 6; ++i) { const float e = (i < 4 || v2) ? __expf(s[i] - mx) : 0.f; s[i] = e; sum += e; }
  const float so = dpp_ror<0x128>(sum);
  sum = hi ? (so + sum) : (sum + so);
  sum = red_sum(sum);
  const float inv = 1.0f / sum;
  const unsigned p0 = pack2(s[0] * inv, s[1] * inv), p1 = pack2(s[2] * inv, s[3] * inv), p2 = pack2(s[4] * inv, s[5] * inv);
  const unsigned o0 = (unsigned)__builtin_amdgcn_update_dpp(0, (int)p0, 0x128, 0xf, 0xf, true);
  const unsigned o1 = (unsigned)__builtin_amdgcn_update_dpp(0, (int)p1, 0x128, 0xf, 0xf, true);
  const unsigned o2 = (unsigned)__builtin_amdgcn_update_dpp(0, (int)p2, 0x128, 0xf, 0xf, true);
  const v4u b0 = {p0, p1, o0, o1}, b1 = {p2, o2, 0u, 0u};
  pb0 = as_frag(b0); pb1 = as_frag(b1);
}

struct Params {
  const u16* ax; const u16* mem0; const u16* Wq; const u16* Wk; const u16* Wv; const u16* Wo;
  const u16* bx; const u16* wqkv; const u16* wo;
  u16* out; char* ws;
};

DI void role_s1(const Params& P, char* smem, int h) {
  int t = threadIdx.x;
  bf16x8 wr[48];
  load_w3(P.Wq, P.Wk, P.Wv, h, smem, wr, t);
  char* ws = P.ws;
  int dead = 0;
  for (int i = t; i < 8 * 16; i += 512) *(v4u*)(smem + S1_K + (40 + (i >> 4)) * KST + (i & 15) * 16) = (v4u){0, 0, 0, 0};
  for (int i = t; i < 128 * 6; i += 512) *(v2u*)(smem + S1_VT + (i / 6) * VST + 80 + (i % 6) * 8) = (v2u){0, 0};
  const float scale = 0.08838834764831845f;
  for (int s = 0; s < NSEG; ++s) {
    asm volatile("" : "+v"(t));
    const int lane = t & 63, w = t >> 6, q = lane >> 4, l15 = lane & 15;
    auto fetch_kv = [&]() {
      wait_word((const unsigned*)(ws + OFF_FLAGKV) + h * 64 + s, 1u, dead, s == 0);
      const char* kx = ws + OFF_KX + ((size_t)(s * NH + h)) * 8192;
      const char* vx = ws + OFF_VXT + ((size_t)(s * NH + h)) * 8192;
      v4u a, b;
      ld16x2_sc1(kx + t * 16, vx + t * 16, a, b);
      *(v4u*)(smem + S1_K + (8 + (t >> 4)) * KST + (t & 15) * 16) = a;
      v2u b0 = {b.x, b.y}, b1 = {b.z, b.w};
      *(v2u*)(smem + S1_VT + (t >> 2) * VST + 16 + (t & 3) * 16) = b0;
      *(v2u*)(smem + S1_VT + (t >> 2) * VST + 16 + (t & 3) * 16 + 8) = b1;
    };
    if (s > 0) fetch_kv();
    {
      const int c0 = t, c1 = 512 + t;
      v4u a, b = {0, 0, 0, 0};
      if (s == 0) {
        a = *(const v4u*)((const char*)P.mem0 + c0 * 16);
        if (w < 2) b = *(const v4u*)((const char*)P.mem0 + c1 * 16);
      } else {
        const char* src = ws + OFF_MEMBUF + (size_t)s * 10240;
        if (S1_PRE > 0) { __builtin_amdgcn_s_sleep(S1_PRE / 2); __builtin_amdgcn_s_sleep(S1_PRE - S1_PRE / 2); }
        {
          const char* prb = src + ((w * 10 + (h % 10)) * 128 + 124);
          for (int sp = 0;; ++sp) {
            unsigned pv = ld4_sc1(prb);
            if (__builtin_amdgcn_readfirstlane(pv) != SENT) break;
            if (dead || sp > SPIN_CAP) { dead = 1; break; }
          }
          if (S1_POST > 0) __builtin_amdgcn_s_sleep(S1_POST);
        }
        for (int sp = 0;; ++sp) {
          bool ok;
          if (w < 2) { ld16x2_sc1(src + c0 * 16, src + c1 * 16, a, b); ok = valid4(a) && valid4(b); }
          else { a = ld16_sc1(src + c0 * 16); ok = valid4(a); }
          if (__all(ok)) break;
          if (dead || sp > SPIN_CAP) { dead = 1; break; }
        }
      }
      if (s == 0) {
        *(v4u*)(smem + S1_A + (c0 / 80) * AST + (c0 % 80) * 16) = a;
        if (w < 2) *(v4u*)(smem + S1_A + (c1 / 80) * AST + (c1 % 80) * 16) = b;
      } else {
        *(v4u*)(smem + S1_A + ((c0 >> 1) & 7) * AST + (c0 >> 4) * 32 + (c0 & 1) * 16) = a;
        if (w < 2) *(v4u*)(smem + S1_A + ((c1 >> 1) & 7) * AST + (c1 >> 4) * 32 + (c1 & 1) * 16) = b;
      }
    }
    __syncthreads();
    f32x4 aq, ak, av;
    proj3_pipe(smem + S1_A, lane & 7, smem, wr, t, aq, ak, av);
    if (q < 2) {
#pragma unroll
      for (int r = 0; r < 4; ++r) {
        *(u16*)(smem + S1_Q + (4 * q + r) * KST + (16 * w + l15) * 2) = f2bf(aq[r]);
        *(u16*)(smem + S1_K + (4 * q + r) * KST + (16 * w + l15) * 2) = f2bf(ak[r]);
      }
      v2u vv = {pack2(av[0], av[1]), pack2(av[2], av[3])};
      *(v2u*)(smem + S1_VT + (16 * w + l15) * VST + q * 8) = vv;
    }
    if (s == 0) fetch_kv();
    __syncthreads();
    if (s < NSEG - 1 && w < 4) {
      const char* qp = smem + S1_Q + (lane & 7) * KST + q * 16;
      const char* kp = smem + S1_K + l15 * KST + q * 16;
      f32x4 st[3];
      st[0] = (f32x4){0.f, 0.f, 0.f, 0.f}; st[1] = st[0]; st[2] = st[0];
      bf16x8 qf0 = lds_frag(qp), ka0 = lds_frag(kp), kb0 = lds_frag(kp + 16 * KST), kc0 = lds_frag(kp + 32 * KST);
      bf16x8 qf1 = lds_frag(qp + 64), ka1 = lds_frag(kp + 64), kb1 = lds_frag(kp + 16 * KST + 64), kc1 = lds_frag(kp + 32 * KST + 64);
      st[0] = mfma16(ka0, qf0, st[0]); st[1] = mfma16(kb0, qf0, st[1]); st[2] = mfma16(kc0, qf0, st[2]);
      qf0 = lds_frag(qp + 128); ka0 = lds_frag(kp + 128); kb0 = lds_frag(kp + 16 * KST + 128); kc0 = lds_frag(kp + 32 * KST + 128);
      st[0] = mfma16(ka1, qf1, st[0]); st[1] = mfma16(kb1, qf1, st[1]); st[2] = mfma16(kc1, qf1, st[2]);
      qf1 = lds_frag(qp + 192); ka1 = lds_frag(kp + 192); kb1 = lds_frag(kp + 16 * KST + 192); kc1 = lds_frag(kp + 32 * KST + 192);
      st[0] = mfma16(ka0, qf0, st[0]); st[1] = mfma16(kb0, qf0, st[1]); st[2] = mfma16(kc0, qf0, st[2]);
      const char* vp = smem + S1_VT + (32 * w + l15) * VST + q * 8;
      s16x4 v0 = *(const s16x4*)(vp), v1 = *(const s16x4*)(vp + 32), v2 = *(const s16x4*)(vp + 64);
      s16x4 u0 = *(const s16x4*)(vp + 16 * VST), u1 = *(const s16x4*)(vp + 16 * VST + 32), u2 = *(const s16x4*)(vp + 16 * VST + 64);
      st[0] = mfma16(ka1, qf1, st[0]); st[1] = mfma16(kb1, qf1, st[1]); st[2] = mfma16(kc1, qf1, st[2]);
      bf16x8 pb0, pb1;
      softmax_mem(st, scale, q, l15 >> 3, pb0, pb1);
      const s16x4 z4 = {0, 0, 0, 0};
      f32x4 o = {0.f, 0.f, 0.f, 0.f}, o2 = o;
      o = mfma16(cat4(v0, v1), pb0, o);
      o2 = mfma16(cat4(u0, u1), pb0, o2);
      o = mfma16(cat4(v2, z4), pb1, o);
      o2 = mfma16(cat4(u2, z4), pb1, o2);
      {
        unsigned o00 = unsent(pack2(o[0], o[1])), o01 = unsent(pack2(o[2], o[3]));
        unsigned o10 = unsent(pack2(o2[0], o2[1])), o11 = unsent(pack2(o2[2], o2[3]));
        auto r0 = __builtin_amdgcn_permlane16_swap(o00, o00, false, false);
        auto r1 = __builtin_amdgcn_permlane16_swap(o01, o01, false, false);
        auto r2 = __builtin_amdgcn_permlane16_swap(o10, o10, false, false);
        auto r3 = __builtin_amdgcn_permlane16_swap(o11, o11, false, false);
        if (l15 < 8) {
          char* dst = ws + OFF_OBUF + (size_t)s * 65536 + h * 2048 + (2 * w + (q & 1)) * 256 + l15 * 32 + (q >> 1) * 16;
          v4u ov = (q & 1) ? (v4u){(unsigned)r2[0], (unsigned)r3[0], (unsigned)r2[1], (unsigned)r3[1]}
                           : (v4u){(unsigned)r0[0], (unsigned)r1[0], (unsigned)r0[1], (unsigned)r1[1]};
          st16_sc1(dst, ov);
        }
      }
    }
    if (t < 128) {
      v4u kv = *(const v4u*)(smem + S1_K + (t >> 4) * KST + (t & 15) * 16);
      st16_sc1(ws + OFF_KM + ((size_t)(s * NH + h)) * 2048 + t * 16, kv);
    } else if (t < 256) {
      const int d = t - 128;
      v2u a = *(const v2u*)(smem + S1_VT + d * VST), b = *(const v2u*)(smem + S1_VT + d * VST + 8);
      v4u vv = {a.x, a.y, b.x, b.y};
      st16_sc1(ws + OFF_VMT + ((size_t)(s * NH + h)) * 2048 + d * 16, vv);
    }
    drain();
    __syncthreads();
    if (t == 0) st4_sc1((unsigned*)(ws + OFF_FLAGM) + h * 64 + s, 1u);
  }
}

DI void load_wo16(const u16* __restrict__ Wo, int c, char* smem, bf16x8 (&wb)[16], int t) {
  const int lane = t & 63, w = t >> 6;
  const u16* src = Wo + (size_t)(512 * w + (lane >> 1)) * DM + 16 * c + 8 * (lane & 1);
  v4u tmp[16];
#pragma unroll
  for (int ks = 0; ks < 16; ++ks) tmp[ks] = *(const v4u*)(src + (size_t)ks * 32 * DM);
  char* base = smem + w * 16384;
#pragma unroll
  for (int ks = 0; ks < 16; ++ks) *(v4u*)(base + ks * 1024 + lane * 16) = tmp[ks];
  __syncthreads();
  const int g = lane >> 4, qq = (lane & 15) >> 2, p = lane & 3;
#pragma unroll
  for (int ks = 0; ks < 16; ++ks) {
    s16x4 lo = tr_read(base + ks * 1024 + (8 * g + qq) * 32 + p * 8);
    s16x4 hi = tr_read(base + ks * 1024 + (8 * g + 4 + qq) * 32 + p * 8);
    wb[ks] = cat4(lo, hi);
  }
  __syncthreads();
}

DI void role_s2(const Params& P, char* smem, int c, int hf) {
  const int t = threadIdx.x, lane = t & 63, w = t >> 6, q = lane >> 4, l15 = lane & 15;
  {
    const int x = blockIdx.x & 7, j = (blockIdx.x - 32) >> 3;
    const int r = t >> 4, c16 = t & 15;
#pragma unroll
    for (int it = 0; it < 3; ++it) {
      v4u d[8];
#pragma unroll
      for (int i = 0; i < 8; ++i) {
        const int u = it * 8 + i;
        const int hh = x + 8 * (u & 3), m = (u >> 2) % 3, g = u / 12;
        const int row = 639 - ((g * 10 + j) * 32 + r);
        const u16* W = m == 0 ? P.Wq : (m == 1 ? P.Wk : P.Wv);
        d[i] = *(const v4u*)(W + (size_t)row * HD + hh * DH + c16 * 8);
      }
#pragma unroll
      for (int i = 0; i < 8; ++i) asm volatile("" :: "v"(d[i]));
    }
  }
  bf16x8 wb[16];
  load_wo16(P.Wo, c, smem, wb, t);
  char* ws = P.ws;
  int dead = 0;
  float* red = (float*)smem;
  for (int s = 0; s < NSEG - 1; ++s) {
    v4u a[4];
    const char* src = ws + OFF_OBUF + (size_t)s * 65536 + w * 8192 + (l15 >> 2) * 2048 + (q >> 1) * 256 + (4 * hf + (l15 & 3)) * 32 + (q & 1) * 16;
    const char* prb = ws + OFF_OBUF + (size_t)s * 65536 + w * 8192 + (c & 31) * 256 + hf * 128 + 124;
    if (S2_PRE > 0 && s > 0) { __builtin_amdgcn_s_sleep(S2_PRE / 2); __builtin_amdgcn_s_sleep(S2_PRE - S2_PRE / 2); }
    for (int sp = 0;; ++sp) {
      unsigned pv = ld4_sc1(prb);
      if (__builtin_amdgcn_readfirstlane(pv) != SENT) break;
      if (dead || sp > SPIN_CAP) { dead = 1; break; }
    }
    if (S2_POST > 0) __builtin_amdgcn_s_sleep(S2_POST);
    {
      bool ok = false;
      for (int sp = 0;; ++sp) {
        if (!ok) {
          asm volatile(
            "global_load_dwordx4 %0, %4, off sc1\n\t"
            "global_load_dwordx4 %1, %4, off offset:512 sc1\n\t"
            "global_load_dwordx4 %2, %4, off offset:1024 sc1\n\t"
            "global_load_dwordx4 %3, %4, off offset:1536 sc1\n\t"
            "s_waitcnt vmcnt(0)"
            : "=&v"(a[0]), "=&v"(a[1]), "=&v"(a[2]), "=&v"(a[3]) : "v"(src) : "memory");
          ok = valid4(a[0]) && valid4(a[1]) && valid4(a[2]) && valid4(a[3]);
        }
        if (__all(ok)) break;
        if (dead || sp > SPIN_CAP) { dead = 1; break; }
      }
    }
    f32x4 acc[4];
#pragma unroll
    for (int kq = 0; kq < 4; ++kq) acc[kq] = (f32x4){0.f, 0.f, 0.f, 0.f};
#pragma unroll
    for (int i = 0; i < 4; ++i)
#pragma unroll
      for (int kq = 0; kq < 4; ++kq) acc[kq] = mfma16(as_frag(a[i]), wb[4 * kq + i], acc[kq]);
    const f32x4 av = (q == 0) ? acc[0] : (q == 1) ? acc[1] : (q == 2) ? acc[2] : acc[3];
    float* rb = red + (s & 1) * 2048 + (w * 4 + q) * 64;
#pragma unroll
    for (int r = 0; r < 4; ++r) rb[r * 16 + l15] = av[r];
    __syncthreads();
    if (t < 256) {
      const int g = t >> 5, pi = t & 31;
      const float* rp = red + (s & 1) * 2048 + pi * 64 + g * 8;
      const f32x4 x0 = *(const f32x4*)rp, x1 = *(const f32x4*)(rp + 4);
      float y[8];
#pragma unroll
      for (int i = 0; i < 4; ++i) { y[i] = row_sum16(x0[i]); y[4 + i] = row_sum16(x1[i]); }
#pragma unroll
      for (int i = 0; i < 8; ++i) {
        unsigned u = __builtin_bit_cast(unsigned, y[i]);
        auto r = __builtin_amdgcn_permlane16_swap(u, u, false, false);
        y[i] = __builtin_bit_cast(float, (unsigned)r[0]) + __builtin_bit_cast(float, (unsigned)r[1]);
      }
      if (pi == 0) {
        v4u o = {unsent(pack2(y[0], y[1])), unsent(pack2(y[2], y[3])), unsent(pack2(y[4], y[5])), unsent(pack2(y[6], y[7]))};
        st16_sc1(ws + OFF_MEMBUF + (size_t)(s + 1) * 10240 + c * 256 + hf * 128 + g * 16, o);
      }
    }
  }
}

DI void role_s4(const Params& P, char* smem, int h, int par) {
  const int t = threadIdx.x, lane = t & 63, w = t >> 6, q = lane >> 4, l15 = lane & 15;
  char* ws = P.ws;
  int dead = 0;
  {
    bf16x8 wr[48];
    load_w3(P.Wq, P.Wk, P.Wv, h, smem, wr, t);
    v4u pa[3];
    auto fetchA = [&](int j) {
      const int row0 = (2 * (j >> 1) + par) * 32 + (j & 1) * 16;
      const char* src = (const char*)P.ax + (size_t)row0 * (DM * 2);
      pa[0] = *(const v4u*)(src + t * 16);
      pa[1] = *(const v4u*)(src + (512 + t) * 16);
      if (t < 256) pa[2] = *(const v4u*)(src + (1024 + t) * 16);
    };
    auto putA = [&](int buf) {
      char* dst = smem + S4_A + buf * 16 * AST;
      *(v4u*)(dst + (t / 80) * AST + (t % 80) * 16) = pa[0];
      *(v4u*)(dst + ((512 + t) / 80) * AST + ((512 + t) % 80) * 16) = pa[1];
      if (t < 256) *(v4u*)(dst + ((1024 + t) / 80) * AST + ((1024 + t) % 80) * 16) = pa[2];
    };
    fetchA(0); putA(0); fetchA(1);
    __syncthreads();
    for (int j = 0; j < 64; ++j) {
      const int seg = 2 * (j >> 1) + par, mt = j & 1;
      f32x4 aq, ak, av;
      proj3(smem + S4_A + (j & 1) * 16 * AST, l15, smem, wr, t, aq, ak, av);
      char* T = smem + S4_T + (j & 1) * 16 * TST;
#pragma unroll
      for (int r = 0; r < 4; ++r) {
        *(u16*)(T + (4 * q + r) * TST + (16 * w + l15) * 2) = f2bf(aq[r]);
        *(u16*)(T + (4 * q + r) * TST + 256 + (16 * w + l15) * 2) = f2bf(ak[r]);
      }
      {
        v2u vv = {pack2(av[0], av[1]), pack2(av[2], av[3])};
        st8_sc1(ws + OFF_VXT + ((size_t)(seg * NH + h)) * 8192 + (16 * w + l15) * 64 + (16 * mt + 4 * q) * 2, vv);
      }
      if (j + 1 < 64) { putA((j + 1) & 1); if (j + 2 < 64) fetchA(j + 2); }
      __syncthreads();
      {
        const int row = t >> 5, ch = t & 31;
        v4u v = *(const v4u*)(T + row * TST + ch * 16);
        const size_t o = ((size_t)(seg * NH + h)) * 8192 + (16 * mt + row) * 256 + (ch & 15) * 16;
        if (ch < 16) *(v4u*)(ws + OFF_QX + o) = v; else st16_sc1(ws + OFF_KX + o, v);
      }
      if (mt == 1) {
        drain();
        __syncthreads();
        if (t == 0) st4_sc1((unsigned*)(ws + OFF_FLAGKV) + h * 64 + seg, 1u);
      }
    }
  }
  const float scale = 0.08838834764831845f;
  const int qt = w & 1, dq = w >> 1;
  for (int i = 0; i < 32; ++i) {
    const int seg = 2 * i + par;
    wait_word((const unsigned*)(ws + OFF_FLAGM) + h * 64 + seg, 1u, dead, seg == NSEG - 1);
    const size_t sh = (size_t)(seg * NH + h);
    const char* km = ws + OFF_KM + sh * 2048;
    const char* vm = ws + OFF_VMT + sh * 2048;
    const char* kx = ws + OFF_KX + sh * 8192;
    const char* vx = ws + OFF_VXT + sh * 8192;
    const char* qx = ws + OFF_QX + sh * 8192;
    v4u kf[3][4], qf[4];
    {
      const char* p0 = (l15 < 8) ? (km + l15 * 256) : (kx + (l15 - 8) * 256);
      const char* p1 = kx + (8 + l15) * 256;
      const char* p2 = kx + (24 + (l15 & 7)) * 256;
      const char* pq = qx + (16 * qt + l15) * 256;
      v4u tmp[16];
      asm volatile(
        "global_load_dwordx4 %0, %16, off sc1\n\t"
        "global_load_dwordx4 %1, %16, off offset:64 sc1\n\t"
        "global_load_dwordx4 %2, %16, off offset:128 sc1\n\t"
        "global_load_dwordx4 %3, %16, off offset:192 sc1\n\t"
        "global_load_dwordx4 %4, %17, off sc1\n\t"
        "global_load_dwordx4 %5, %17, off offset:64 sc1\n\t"
        "global_load_dwordx4 %6, %17, off offset:128 sc1\n\t"
        "global_load_dwordx4 %7, %17, off offset:192 sc1\n\t"
        "global_load_dwordx4 %8, %18, off sc1\n\t"
        "global_load_dwordx4 %9, %18, off offset:64 sc1\n\t"
        "global_load_dwordx4 %10, %18, off offset:128 sc1\n\t"
        "global_load_dwordx4 %11, %18, off offset:192 sc1\n\t"
        "global_load_dwordx4 %12, %19, off sc1\n\t"
        "global_load_dwordx4 %13, %19, off offset:64 sc1\n\t"
        "global_load_dwordx4 %14, %19, off offset:128 sc1\n\t"
        "global_load_dwordx4 %15, %19, off offset:192 sc1\n\t"
        "s_waitcnt vmcnt(0)"
        : "=&v"(tmp[0]), "=&v"(tmp[1]), "=&v"(tmp[2]), "=&v"(tmp[3]), "=&v"(tmp[4]), "=&v"(tmp[5]), "=&v"(tmp[6]), "=&v"(tmp[7]),
          "=&v"(tmp[8]), "=&v"(tmp[9]), "=&v"(tmp[10]), "=&v"(tmp[11]), "=&v"(tmp[12]), "=&v"(tmp[13]), "=&v"(tmp[14]), "=&v"(tmp[15])
        : "v"(p0 + q * 16), "v"(p1 + q * 16), "v"(p2 + q * 16), "v"(pq + q * 16) : "memory");
#pragma unroll
      for (int ks = 0; ks < 4; ++ks) { kf[0][ks] = tmp[ks]; kf[1][ks] = tmp[4 + ks]; kf[2][ks] = tmp[8 + ks]; qf[ks] = tmp[12 + ks]; }
    }
    f32x4 st[3];
#pragma unroll
    for (int kt = 0; kt < 3; ++kt) {
      st[kt] = (f32x4){0.f, 0.f, 0.f, 0.f};
#pragma unroll
      for (int ks = 0; ks < 4; ++ks) st[kt] = mfma16(as_frag(kf[kt][ks]), as_frag(qf[ks]), st[kt]);
    }
    const int qi = 16 * qt + l15;
    bf16x8 pb0, pb1;
    softmax_pt(st, scale, [&](int kt, int r) { const int key = 16 * kt + 4 * q + r; return key < 8 || (key < 40 && key - 8 <= qi); }, pb0, pb1);
    unsigned ok[2][2];
#pragma unroll
    for (int dd = 0; dd < 2; ++dd) {
      const int d = 16 * (2 * dq + dd) + l15;
      const char* pa = (q < 2) ? (vm + d * 16 + q * 8) : (vx + d * 64 + (4 * q - 8) * 2);
      const char* pb = vx + d * 64 + (8 + 4 * q) * 2;
      const char* pc = vx + d * 64 + (24 + 4 * (q & 1)) * 2;
      v2u a, b, c;
      asm volatile("global_load_dwordx2 %0, %3, off sc1\n\tglobal_load_dwordx2 %1, %4, off sc1\n\tglobal_load_dwordx2 %2, %5, off sc1\n\ts_waitcnt vmcnt(0)"
                   : "=&v"(a), "=&v"(b), "=&v"(c) : "v"(pa), "v"(pb), "v"(pc) : "memory");
      if (q >= 2) c = (v2u){0, 0};
      v4u f0 = {a.x, a.y, b.x, b.y}, f1 = {c.x, c.y, 0, 0};
      f32x4 o = {0.f, 0.f, 0.f, 0.f};
      o = mfma16(as_frag(f0), pb0, o);
      o = mfma16(as_frag(f1), pb1, o);
      ok[dd][0] = pack2(o[0], o[1]); ok[dd][1] = pack2(o[2], o[3]);
    }
    {
      auto r0 = __builtin_amdgcn_permlane16_swap(ok[0][0], ok[0][0], false, false);
      auto r1 = __builtin_amdgcn_permlane16_swap(ok[0][1], ok[0][1], false, false);
      auto r2 = __builtin_amdgcn_permlane16_swap(ok[1][0], ok[1][0], false, false);
      auto r3 = __builtin_amdgcn_permlane16_swap(ok[1][1], ok[1][1], false, false);
      char* dst = ws + OFF_OX + ((size_t)((seg * 2 + qt) * NH + h)) * 4096 + (2 * dq + (q & 1)) * 512 + l15 * 32 + (q >> 1) * 16;
      v4u ov = (q & 1) ? (v4u){(unsigned)r2[0], (unsigned)r3[0], (unsigned)r2[1], (unsigned)r3[1]}
                       : (v4u){(unsigned)r0[0], (unsigned)r1[0], (unsigned)r0[1], (unsigned)r1[1]};
      st16_sc1(dst, ov);
    }
    drain();
    __syncthreads();
    if (t == 0) __hip_atomic_fetch_add((unsigned*)(ws + OFF_CNTOX + seg * 64), 1u, __ATOMIC_RELAXED, __HIP_MEMORY_SCOPE_AGENT);
  }
}

DI void role_s3(const Params& P, char* smem, int c, int par) {
  const int t = threadIdx.x, lane = t & 63, w = t >> 6, q = lane >> 4, l15 = lane & 15;
  bf16x8 wb0[16], wb1[16];
  load_wo16(P.Wo, 2 * c, smem, wb0, t);
  load_wo16(P.Wo, 2 * c + 1, smem, wb1, t);
  char* ws = P.ws;
  int dead = 0;
  float* red = (float*)smem;
  for (int i = 0; i < 32; ++i) {
    const int seg = 2 * i + par;
    wait_word((const unsigned*)(ws + OFF_CNTOX + seg * 64), 31u, dead, seg == NSEG - 1);
    float* rb = red + (i & 1) * 8192 + w * 1024;
#pragma unroll
    for (int mt = 0; mt < 2; ++mt) {
      f32x4 acc0 = {0.f, 0.f, 0.f, 0.f}, acc1 = acc0;
#pragma unroll
      for (int hf = 0; hf < 2; ++hf) {
        v4u a[8];
        ld16x8s1k_sc1(ws + OFF_OX + ((size_t)((seg * 2 + mt) * NH + 4 * w)) * 4096 + hf * 8192 + (q >> 1) * 512 + l15 * 32 + (q & 1) * 16, a);
#pragma unroll
        for (int ks = 0; ks < 8; ++ks) {
          acc0 = mfma16(as_frag(a[ks]), wb0[hf * 8 + ks], acc0);
          acc1 = mfma16(as_frag(a[ks]), wb1[hf * 8 + ks], acc1);
        }
        if (seg < NSEG - 1) __builtin_amdgcn_s_sleep(S3_SLEEP);
      }
#pragma unroll
      for (int r = 0; r < 4; ++r) { rb[(16 * mt + 4 * q + r) * 32 + l15] = acc0[r]; rb[(16 * mt + 4 * q + r) * 32 + 16 + l15] = acc1[r]; }
    }
    __syncthreads();
    {
      const int row = t >> 4, cp = t & 15;
      const float* rp = red + (i & 1) * 8192 + row * 32 + 2 * cp;
      float x0 = 0.f, x1 = 0.f;
#pragma unroll
      for (int ww = 0; ww < 8; ++ww) { x0 += rp[ww * 1024]; x1 += rp[ww * 1024 + 1]; }
      *(unsigned*)(P.out + (size_t)(seg * 32 + row) * OUTW + 32 * c + 2 * cp) = pack2(x0, x1);
    }
  }
}

constexpr int PB_A = 0;
constexpr int PB_R = 2 * 16 * AST;
constexpr int RST = 496;
constexpr int PB_O = PB_R + 3 * 16 * RST;
constexpr int OST = 176;
constexpr int PB_STG = 81920;
DI void role_pb(const Params& P, char* smem, int g, int rb) {
  const int t = threadIdx.x, lane = t & 63, w = t >> 6, q = lane >> 4, l15 = lane & 15;
  char* ws = P.ws;
  int dead = 0;
  const int gg = lane >> 4, qq = (lane & 15) >> 2, pp = lane & 3;
  {
    bf16x8 wb[40];
#pragma unroll
    for (int nn = 0; nn < 2; ++nn) {
      const int n = (2 * w + nn) < 15 ? (2 * w + nn) : 14;
      const int col0 = (n / 5) * DM + 80 * g + 16 * (n % 5);
      const u16* src = P.wqkv + (size_t)(lane >> 1) * 1920 + col0 + 8 * (lane & 1);
#pragma unroll
      for (int half = 0; half < 2; ++half) {
        v4u tmp[10];
#pragma unroll
        for (int i = 0; i < 10; ++i) tmp[i] = *(const v4u*)(src + (size_t)(half * 10 + i) * 32 * 1920);
        char* base = smem + PB_STG + w * 10240;
#pragma unroll
        for (int i = 0; i < 10; ++i) *(v4u*)(base + i * 1024 + lane * 16) = tmp[i];
        __syncthreads();
#pragma unroll
        for (int i = 0; i < 10; ++i) {
          s16x4 lo = tr_read(base + i * 1024 + (8 * gg + qq) * 32 + pp * 8);
          s16x4 hi = tr_read(base + i * 1024 + (8 * gg + 4 + qq) * 32 + pp * 8);
          wb[nn * 20 + half * 10 + i] = cat4(lo, hi);
        }
        __syncthreads();
      }
    }
    v4u pa[3];
    auto fetchA = [&](int j) {
      const int row0 = 512 * rb - 16 + 16 * j;
      if (row0 < 0) { pa[0] = (v4u){0, 0, 0, 0}; pa[1] = pa[0]; pa[2] = pa[0]; return; }
      const char* src = (const char*)P.bx + (size_t)row0 * (DM * 2);
      pa[0] = *(const v4u*)(src + t * 16);
      pa[1] = *(const v4u*)(src + (512 + t) * 16);
      if (t < 256) pa[2] = *(const v4u*)(src + (1024 + t) * 16);
    };
    auto putA = [&](int buf) {
      char* dst = smem + PB_A + buf * 16 * AST;
      *(v4u*)(dst + (t / 80) * AST + (t % 80) * 16) = pa[0];
      *(v4u*)(dst + ((512 + t) / 80) * AST + ((512 + t) % 80) * 16) = pa[1];
      if (t < 256) *(v4u*)(dst + ((1024 + t) / 80) * AST + ((1024 + t) % 80) * 16) = pa[2];
    };
    fetchA(0); putA(0); fetchA(1);
    __syncthreads();
    const float scale = 0.22360679774997896f;
    for (int j = 0; j < 33; ++j) {
      const char* ap = smem + PB_A + (j & 1) * 16 * AST + l15 * AST + q * 16;
      f32x4 c0 = {0.f, 0.f, 0.f, 0.f}, c1 = c0;
#pragma unroll
      for (int ks = 0; ks < 20; ++ks) {
        bf16x8 a = lds_frag(ap + ks * 64);
        c0 = mfma16(a, wb[ks], c0);
        c1 = mfma16(a, wb[20 + ks], c1);
      }
      char* R = smem + PB_R + (j % 3) * 16 * RST;
#pragma unroll
      for (int r = 0; r < 4; ++r) {
        *(u16*)(R + (4 * q + r) * RST + (32 * w + l15) * 2) = f2bf(c0[r]);
        if (w < 7) *(u16*)(R + (4 * q + r) * RST + (32 * w + 16 + l15) * 2) = f2bf(c1[r]);
      }
      if (j + 1 < 33) { putA((j + 1) & 1); if (j + 2 < 33) fetchA(j + 2); }
      __syncthreads();
      if (j >= 2 && t < 160) {
        const int row = t / 10, ch = t % 10;
        v4u v = *(const v4u*)(smem + PB_O + ((j - 1) & 1) * 16 * OST + row * OST + ch * 16);
        st16_sc1(ws + OFF_OB + (size_t)(512 * rb - 16 + 16 * (j - 1) + row) * 1280 + (80 * g) * 2 + ch * 16, v);
      }
      if (j >= 1) {
        const int item = t >> 3, wi = t & 7, row = item >> 2, hh = item & 3;
        const int srow = 512 * rb - 16 + 16 * j + row;
        const int krow = row - 2 * wi;
        const bool vis = (srow - 2 * wi) >= 0;
        const char* Rc = smem + PB_R + (j % 3) * 16 * RST;
        const char* Rp = smem + PB_R + ((j + 2) % 3) * 16 * RST;
        const char* qrow = Rc + row * RST + (20 * hh) * 2;
        const char* krowp = (krow >= 0 ? Rc + krow * RST : Rp + (krow + 16) * RST);
        float dot = 0.f;
#pragma unroll
        for (int d = 0; d < 20; d += 2) {
          unsigned qv = *(const unsigned*)(qrow + d * 2);
          unsigned kv = *(const unsigned*)(krowp + (80 + 20 * hh + d) * 2);
          dot += bf2f((u16)qv) * bf2f((u16)kv);
          dot += bf2f((u16)(qv >> 16)) * bf2f((u16)(kv >> 16));
        }
        float lg = rbf(rbf(dot) * scale);
        float mx = vis ? lg : -3.0e38f;
        mx = fmaxf(mx, __shfl_xor(mx, 1)); mx = fmaxf(mx, __shfl_xor(mx, 2)); mx = fmaxf(mx, __shfl_xor(mx, 4));
        float e = vis ? __expf(lg - mx) : 0.f;
        float sum = e;
        sum += __shfl_xor(sum, 1); sum += __shfl_xor(sum, 2); sum += __shfl_xor(sum, 4);
        const float pw = rbf(e * (1.0f / sum));
        float o0 = 0.f, o1 = 0.f, o2 = 0.f;
#pragma unroll
        for (int ww = 0; ww < 8; ++ww) {
          const float pv = __shfl(pw, (lane & ~7) + ww);
          const int kr = row - 2 * ww;
          const char* vr = (kr >= 0 ? Rc + kr * RST : Rp + (kr + 16) * RST) + (160 + 20 * hh) * 2;
          o0 += pv * bf2f(*(const u16*)(vr + wi * 2));
          o1 += pv * bf2f(*(const u16*)(vr + (wi + 8) * 2));
          if (wi < 4) o2 += pv * bf2f(*(const u16*)(vr + (wi + 16) * 2));
        }
        char* O = smem + PB_O + (j & 1) * 16 * OST + row * OST + (20 * hh) * 2;
        *(u16*)(O + wi * 2) = f2bf(o0);
        *(u16*)(O + (wi + 8) * 2) = f2bf(o1);
        if (wi < 4) *(u16*)(O + (wi + 16) * 2) = f2bf(o2);
      }
    }
    __syncthreads();
    if (t < 160) {
      const int row = t / 10, ch = t % 10;
      v4u v = *(const v4u*)(smem + PB_O + (32 & 1) * 16 * OST + row * OST + ch * 16);
      st16_sc1(ws + OFF_OB + (size_t)(512 * rb - 16 + 16 * 32 + row) * 1280 + (80 * g) * 2 + ch * 16, v);
    }
    drain();
    __syncthreads();
    if (t == 0) __hip_atomic_fetch_add((unsigned*)(ws + OFF_CNTB + rb * 64), 1u, __ATOMIC_RELAXED, __HIP_MEMORY_SCOPE_AGENT);
  }
  {
    char* stg = smem + 102400 + w * 7168;
    for (int f0 = w * 13; f0 < w * 13 + 13 && f0 < 100; f0 += 7) {
      v4u tmp[7];
#pragma unroll
      for (int i = 0; i < 7; ++i) {
        const int f = f0 + i; const bool okf = (f < w * 13 + 13) && (f < 100);
        const int ff = okf ? f : 0;
        tmp[i] = *(const v4u*)(P.wo + (size_t)((ff % 20) * 32 + (lane >> 1)) * DM + 80 * g + 16 * (ff / 20) + 8 * (lane & 1));
      }
#pragma unroll
      for (int i = 0; i < 7; ++i) *(v4u*)(stg + i * 1024 + lane * 16) = tmp[i];
      __builtin_amdgcn_s_waitcnt(0xC07F);
#pragma unroll
      for (int i = 0; i < 7; ++i) {
        const int f = f0 + i; const bool okf = (f < w * 13 + 13) && (f < 100);
        s16x4 lo = tr_read(stg + i * 1024 + (8 * gg + qq) * 32 + pp * 8);
        s16x4 hi = tr_read(stg + i * 1024 + (8 * gg + 4 + qq) * 32 + pp * 8);
        if (okf) *(bf16x8*)(smem + f * 1024 + lane * 16) = cat4(lo, hi);
      }
      __builtin_amdgcn_s_waitcnt(0xC07F);
    }
    __syncthreads();
    wait_word((const unsigned*)(ws + OFF_CNTB + rb * 64), 7u, dead);
    for (int i = 0; i < 4; ++i) {
      const int row0 = 512 * rb + 16 * (w + 8 * i);
      v4u a0[10], a1[10];
      ld16x10_sc1(ws + OFF_OB + (size_t)(row0 + l15) * 1280 + q * 16, a0);
      ld16x10_sc1(ws + OFF_OB + (size_t)(row0 + l15) * 1280 + 640 + q * 16, a1);
#pragma unroll
      for (int n = 0; n < 5; ++n) {
        f32x4 acc = {0.f, 0.f, 0.f, 0.f};
#pragma unroll
        for (int ks = 0; ks < 10; ++ks) acc = mfma16(as_frag(a0[ks]), lds_frag(smem + (n * 20 + ks) * 1024 + lane * 16), acc);
#pragma unroll
        for (int ks = 0; ks < 10; ++ks) acc = mfma16(as_frag(a1[ks]), lds_frag(smem + (n * 20 + 10 + ks) * 1024 + lane * 16), acc);
#pragma unroll
        for (int r = 0; r < 4; ++r) P.out[(size_t)(row0 + 4 * q + r) * OUTW + DM + 80 * g + 16 * n + l15] = f2bf(acc[r]);
      }
    }
  }
}

__global__ __launch_bounds__(512, 2) void fused_kernel(Params P) {
  extern __shared__ __attribute__((aligned(16))) char smem[];
  const int b = blockIdx.x;
  if (b < 32) role_s1(P, smem, b);
  else if (b < 112) role_s2(P, smem, (b - 32) % 40, (b - 32) / 40);
  else if (b < 176) role_s4(P, smem, (b - 112) & 31, (b - 112) >> 5);
  else if (b < 216) role_s3(P, smem, (b - 176) % 20, (b - 176) / 20);
  else if (b < 248) role_pb(P, smem, (b - 216) & 7, (b - 216) >> 3);
}

extern "C" void kernel_launch(void* const* d_in, const int* in_sizes, int n_in,
                              void* d_out, int out_size, void* d_ws, size_t ws_size,
                              hipStream_t stream) {
  Params P;
  P.ax = (const u16*)d_in[0]; P.mem0 = (const u16*)d_in[1];
  P.Wq = (const u16*)d_in[2]; P.Wk = (const u16*)d_in[3]; P.Wv = (const u16*)d_in[4]; P.Wo = (const u16*)d_in[5];
  P.bx = (const u16*)d_in[7]; P.wqkv = (const u16*)d_in[8]; P.wo = (const u16*)d_in[9];
  P.out = (u16*)d_out; P.ws = (char*)d_ws;
  hipFuncSetAttribute((const void*)fused_kernel, hipFuncAttributeMaxDynamicSharedMemorySize, LDS_BYTES);
  hipMemsetAsync(d_ws, 0xFF, POISON_BYTES, stream);
  fused_kernel<<<256, 512, LDS_BYTES, stream>>>(P);
}
```

```cpp
#include <hip/hip_runtime.h>
#include <hip/hip_bf16.h>
#include <stdint.h>

typedef unsigned short u16;
typedef short bf16x8 __attribute__((ext_vector_type(8)));
typedef short s16x4 __attribute__((ext_vector_type(4)));
typedef float f32x4 __attribute__((ext_vector_type(4)));
typedef unsigned v4u __attribute__((ext_vector_type(4)));
typedef unsigned v2u __attribute__((ext_vector_type(2)));
#define LAS __attribute__((address_space(3)))
#define DI __device__ __forceinline__

constexpr int SLEN = 2048, DM = 640, NH = 32, DH = 128, HD = 4096, NSEG = 64;
constexpr int OUTW = 1280;
constexpr size_t OFF_FLAGKV = 0;
constexpr size_t OFF_FLAGM  = 8192;
constexpr size_t OFF_CNTOX  = 16384;
constexpr size_t OFF_CNTB   = 20480;
constexpr size_t OFF_MEMBUF = 24576;
constexpr size_t OFF_OBUF   = OFF_MEMBUF + 64 * 10240;
constexpr size_t POISON_BYTES = OFF_OBUF + (size_t)64 * 65536;
constexpr size_t OFF_KX  = (size_t)8 << 20;
constexpr size_t OFF_VXT = (size_t)24 << 20;
constexpr size_t OFF_QX  = (size_t)40 << 20;
constexpr size_t OFF_KM  = (size_t)56 << 20;
constexpr size_t OFF_VMT = (size_t)60 << 20;
constexpr size_t OFF_OX  = (size_t)64 << 20;
constexpr size_t OFF_OB  = (size_t)80 << 20;
constexpr unsigned SENT = 0xFFFFFFFFu;
constexpr int SPIN_CAP = 300000;
constexpr int LDS_BYTES = 160 * 1024;
#define POLL_SLEEP 60
#define S1_PRE 28
#define S2_PRE 76
#define S1_POST 0
#define S2_POST 0
#define S3_SLEEP 8

DI float bf2f(u16 b) { return __builtin_bit_cast(float, (unsigned)b << 16); }
DI u16 f2bf(float f) { __bf16 b = (__bf16)f; return __builtin_bit_cast(u16, b); }
DI float rbf(float f) { return bf2f(f2bf(f)); }
DI unsigned pack2(float a, float b) { return (unsigned)f2bf(a) | ((unsigned)f2bf(b) << 16); }
DI unsigned unsent(unsigned x) { return x == SENT ? 0xFFFEFFFFu : x; }
DI bool valid4(v4u v) { return v.x != SENT && v.y != SENT && v.z != SENT && v.w != SENT; }
DI f32x4 mfma16(bf16x8 a, bf16x8 b, f32x4 c) { return __builtin_amdgcn_mfma_f32_16x16x32_bf16(a, b, c, 0, 0, 0); }
DI bf16x8 as_frag(v4u v) { return __builtin_bit_cast(bf16x8, v); }
DI s16x4 tr_read(const char* p) {
  typedef short v4i16_t __attribute__((ext_vector_type(4)));
  return __builtin_amdgcn_ds_read_tr16_b64_v4i16((LAS v4i16_t*)(p));
}
DI float red_max(float x) {
  unsigned u = __builtin_bit_cast(unsigned, x);
  auto r = __builtin_amdgcn_permlane16_swap(u, u, false, false);
  x = fmaxf(__builtin_bit_cast(float, (unsigned)r[0]), __builtin_bit_cast(float, (unsigned)r[1]));
  u = __builtin_bit_cast(unsigned, x);
  auto r2 = __builtin_amdgcn_permlane32_swap(u, u, false, false);
  return fmaxf(__builtin_bit_cast(float, (unsigned)r2[0]), __builtin_bit_cast(float, (unsigned)r2[1]));
}
DI float red_sum(float x) {
  unsigned u = __builtin_bit_cast(unsigned, x);
  auto r = __builtin_amdgcn_permlane16_swap(u, u, false, false);
  x = __builtin_bit_cast(float, (unsigned)r[0]) + __builtin_bit_cast(float, (unsigned)r[1]);
  u = __builtin_bit_cast(unsigned, x);
  auto r2 = __builtin_amdgcn_permlane32_swap(u, u, false, false);
  return __builtin_bit_cast(float, (unsigned)r2[0]) + __builtin_bit_cast(float, (unsigned)r2[1]);
}
DI bf16x8 cat4(s16x4 lo, s16x4 hi) { bf16x8 r; r[0]=lo[0]; r[1]=lo[1]; r[2]=lo[2]; r[3]=lo[3]; r[4]=hi[0]; r[5]=hi[1]; r[6]=hi[2]; r[7]=hi[3]; return r; }
DI bf16x8 lds_frag(const char* p) { return *(const bf16x8*)p; }

DI v4u ld16_sc1(const void* p) { v4u r; asm volatile("global_load_dwordx4 %0, %1, off sc1\n\ts_waitcnt vmcnt(0)" : "=&v"(r) : "v"(p) : "memory"); return r; }
DI void ld16x2_sc1(const void* p0, const void* p1, v4u& a, v4u& b) {
  asm volatile("global_load_dwordx4 %0, %2, off sc1\n\tglobal_load_dwordx4 %1, %3, off sc1\n\ts_waitcnt vmcnt(0)"
               : "=&v"(a), "=&v"(b) : "v"(p0), "v"(p1) : "memory");
}
DI unsigned ld4_sc1(const void* p) { unsigned r; asm volatile("global_load_dword %0, %1, off sc1\n\ts_waitcnt vmcnt(0)" : "=&v"(r) : "v"(p) : "memory"); return r; }
DI void st16_sc1(void* p, v4u v) { asm volatile("global_store_dwordx4 %0, %1, off sc1\n\ts_nop 1" :: "v"(p), "v"(v) : "memory"); }
DI void st8_sc1(void* p, v2u v) { asm volatile("global_store_dwordx2 %0, %1, off sc1\n\ts_nop 1" :: "v"(p), "v"(v) : "memory"); }
DI void st4_sc1(void* p, unsigned v) { asm volatile("global_store_dword %0, %1, off sc1\n\ts_nop 1" :: "v"(p), "v"(v) : "memory"); }
DI float dpp_xor1(float x) { return __builtin_bit_cast(float, __builtin_amdgcn_update_dpp(0, __builtin_bit_cast(int, x), 0xB1, 0xf, 0xf, true)); }
DI float dpp_xor2(float x) { return __builtin_bit_cast(float, __builtin_amdgcn_update_dpp(0, __builtin_bit_cast(int, x), 0x4E, 0xf, 0xf, true)); }
DI float dpp_shl4(float x) { return __builtin_bit_cast(float, __builtin_amdgcn_update_dpp(0, __builtin_bit_cast(int, x), 0x104, 0xf, 0xf, true)); }
template <int CTRL> DI float dpp_ror(float x) { return __builtin_bit_cast(float, __builtin_amdgcn_update_dpp(0, __builtin_bit_cast(int, x), CTRL, 0xf, 0xf, true)); }
DI float row_sum16(float x) { x += dpp_ror<0x128>(x); x += dpp_ror<0x124>(x); x += dpp_ror<0x122>(x); x += dpp_ror<0x121>(x); return x; }
DI void drain() { asm volatile("s_waitcnt vmcnt(0)" ::: "memory"); }
DI void ld16x8s1k_sc1(const char* p, v4u (&a)[8]) {
  asm volatile(
    "global_load_dwordx4 %0, %8, off sc1\n\t"
    "global_load_dwordx4 %1, %8, off offset:1024 sc1\n\t"
    "global_load_dwordx4 %2, %8, off offset:2048 sc1\n\t"
    "global_load_dwordx4 %3, %8, off offset:3072 sc1\n\t"
    "global_load_dwordx4 %4, %9, off sc1\n\t"
    "global_load_dwordx4 %5, %9, off offset:1024 sc1\n\t"
    "global_load_dwordx4 %6, %9, off offset:2048 sc1\n\t"
    "global_load_dwordx4 %7, %9, off offset:3072 sc1\n\t"
    "s_waitcnt vmcnt(0)"
    : "=&v"(a[0]), "=&v"(a[1]), "=&v"(a[2]), "=&v"(a[3]), "=&v"(a[4]), "=&v"(a[5]), "=&v"(a[6]), "=&v"(a[7])
    : "v"(p), "v"(p + 4096) : "memory");
}
DI void ld16x8s512_sc1(const char* p, v4u (&a)[8]) {
  asm volatile(
    "global_load_dwordx4 %0, %8, off sc1\n\t"
    "global_load_dwordx4 %1, %8, off offset:512 sc1\n\t"
    "global_load_dwordx4 %2, %8, off offset:1024 sc1\n\t"
    "global_load_dwordx4 %3, %8, off offset:1536 sc1\n\t"
    "global_load_dwordx4 %4, %8, off offset:2048 sc1\n\t"
    "global_load_dwordx4 %5, %8, off offset:2560 sc1\n\t"
    "global_load_dwordx4 %6, %8, off offset:3072 sc1\n\t"
    "global_load_dwordx4 %7, %8, off offset:3584 sc1\n\t"
    "s_waitcnt vmcnt(0)"
    : "=&v"(a[0]), "=&v"(a[1]), "=&v"(a[2]), "=&v"(a[3]), "=&v"(a[4]), "=&v"(a[5]), "=&v"(a[6]), "=&v"(a[7])
    : "v"(p) : "memory");
}
DI void ld16x10_sc1(const char* p, v4u (&a)[10]) {
  asm volatile(
    "global_load_dwordx4 %0, %10, off sc1\n\t"
    "global_load_dwordx4 %1, %10, off offset:64 sc1\n\t"
    "global_load_dwordx4 %2, %10, off offset:128 sc1\n\t"
    "global_load_dwordx4 %3, %10, off offset:192 sc1\n\t"
    "global_load_dwordx4 %4, %10, off offset:256 sc1\n\t"
    "global_load_dwordx4 %5, %10, off offset:320 sc1\n\t"
    "global_load_dwordx4 %6, %10, off offset:384 sc1\n\t"
    "global_load_dwordx4 %7, %10, off offset:448 sc1\n\t"
    "global_load_dwordx4 %8, %10, off offset:512 sc1\n\t"
    "global_load_dwordx4 %9, %10, off offset:576 sc1\n\t"
    "s_waitcnt vmcnt(0)"
    : "=&v"(a[0]), "=&v"(a[1]), "=&v"(a[2]), "=&v"(a[3]), "=&v"(a[4]), "=&v"(a[5]), "=&v"(a[6]), "=&v"(a[7]),
      "=&v"(a[8]), "=&v"(a[9])
    : "v"(p) : "memory");
}
DI void wait_word(const unsigned* p, unsigned want, int& dead, bool fast = false) {
  if ((threadIdx.x >> 6) == 0 && !dead) {
    int sp = 0;
    for (; sp < SPIN_CAP; ++sp) {
      unsigned v = ld4_sc1(p);
      if (__builtin_amdgcn_readfirstlane(v) == want) break;
      if (fast) __builtin_amdgcn_s_sleep(4); else __builtin_amdgcn_s_sleep(POLL_SLEEP);
    }
    if (sp >= SPIN_CAP) dead = 1;
  }
  __syncthreads();
}

constexpr int AST = 1296;
constexpr int KST = 272;
constexpr int VST = 136;
constexpr int L_WRES = 0;
constexpr int L_STG = 98304;
constexpr int STG_BUF = 3 * 32 * KST;
constexpr int S1_A = 98304;
constexpr int S1_K = S1_A + 10496;
constexpr int S1_VT = S1_K + 48 * KST;
constexpr int S1_Q = S1_VT + 128 * VST;
constexpr int S4_A = 98304;
constexpr int S4_T = S4_A + 2 * 16 * AST;
constexpr int TST = 528;

DI void load_w3(const u16* __restrict__ Wq, const u16* __restrict__ Wk, const u16* __restrict__ Wv, int h,
                char* smem, bf16x8 (&wr)[48], int t) {
  const int lane = t & 63, w = t >> 6;
  const int r = t >> 4, c16 = t & 15;
  const size_t goff = (size_t)r * HD + h * DH + c16 * 8;
  const int g = lane >> 4, qq = (lane & 15) >> 2, p = lane & 3;
  const int troff = (8 * g + qq) * KST + (16 * w + 4 * p) * 2;
  constexpr int PD = 5;
  v4u pf[PD][3];
#pragma unroll
  for (int i = 0; i < PD; ++i) {
    pf[i][0] = *(const v4u*)(Wq + goff + (size_t)i * 32 * HD);
    pf[i][1] = *(const v4u*)(Wk + goff + (size_t)i * 32 * HD);
    pf[i][2] = *(const v4u*)(Wv + goff + (size_t)i * 32 * HD);
  }
#pragma unroll
  for (int ks = 0; ks < 20; ++ks) {
    char* stg = smem + L_STG + (ks & 1) * STG_BUF;
#pragma unroll
    for (int m = 0; m < 3; ++m) *(v4u*)(stg + m * 32 * KST + r * KST + c16 * 16) = pf[ks % PD][m];
    if (ks + PD < 20) {
      pf[ks % PD][0] = *(const v4u*)(Wq + goff + (size_t)(ks + PD) * 32 * HD);
      pf[ks % PD][1] = *(const v4u*)(Wk + goff + (size_t)(ks + PD) * 32 * HD);
      pf[ks % PD][2] = *(const v4u*)(Wv + goff + (size_t)(ks + PD) * 32 * HD);
    }
    __syncthreads();
#pragma unroll
    for (int m = 0; m < 3; ++m) {
      s16x4 lo = tr_read(stg + m * 32 * KST + troff);
      s16x4 hi = tr_read(stg + m * 32 * KST + troff + 4 * KST);
      bf16x8 f = cat4(lo, hi);
      if (ks < 16) wr[ks * 3 + m] = f;
      else *(bf16x8*)(smem + L_WRES + w * 12288 + ((ks - 16) * 3 + m) * 1024 + lane * 16) = f;
    }
  }
  __syncthreads();
}

DI void proj3(const char* ldsA, int arow, const char* smem, const bf16x8 (&wr)[48], int t, f32x4& aq, f32x4& ak, f32x4& av) {
  const int lane = t & 63, w = t >> 6, q = lane >> 4;
  const char* ap = ldsA + arow * AST + q * 16;
  int woff = w * 12288 + lane * 16;
  asm volatile("" : "+v"(woff));
  const char* wp = smem + L_WRES + woff;
  aq = (f32x4){0.f, 0.f, 0.f, 0.f}; ak = aq; av = aq;
#pragma unroll
  for (int ks = 0; ks < 20; ++ks) {
    bf16x8 a = lds_frag(ap + ks * 64);
    bf16x8 b0, b1, b2;
    if (ks < 16) { b0 = wr[ks * 3]; b1 = wr[ks * 3 + 1]; b2 = wr[ks * 3 + 2]; }
    else { b0 = lds_frag(wp + ((ks - 16) * 3) * 1024); b1 = lds_frag(wp + ((ks - 16) * 3 + 1) * 1024); b2 = lds_frag(wp + ((ks - 16) * 3 + 2) * 1024); }
    aq = mfma16(a, b0, aq);
    ak = mfma16(a, b1, ak);
    av = mfma16(a, b2, av);
    if ((ks & 3) == 3) { asm volatile("" : "+v"(aq), "+v"(ak), "+v"(av)); __builtin_amdgcn_sched_barrier(0); }
  }
}

DI void proj3_pipe(const char* ldsA, int arow, const char* smem, const bf16x8 (&wr)[48], int t, f32x4& aq, f32x4& ak, f32x4& av) {
  const int lane = t & 63, w = t >> 6, q = lane >> 4;
  const char* ap = ldsA + arow * AST + q * 16;
  int woff = w * 12288 + lane * 16;
  asm volatile("" : "+v"(woff));
  const char* wp = smem + L_WRES + woff;
  aq = (f32x4){0.f, 0.f, 0.f, 0.f}; ak = aq; av = aq;
  bf16x8 a[2][4];
#pragma unroll
  for (int i = 0; i < 4; ++i) a[0][i] = lds_frag(ap + ((i < 3) ? i : 16) * 64);
#pragma unroll
  for (int g = 0; g < 5; ++g) {
    bf16x8 b0, b1, b2;
    if (g < 4) { b0 = lds_frag(wp + (g * 3) * 1024); b1 = lds_frag(wp + (g * 3 + 1) * 1024); b2 = lds_frag(wp + (g * 3 + 2) * 1024); }
    if (g < 4) {
#pragma unroll
      for (int i = 0; i < 4; ++i) {
        const int ks = (g + 1 < 4) ? ((i < 3) ? 3 * (g + 1) + i : 16 + g + 1) : 12 + i;
        a[(g + 1) & 1][i] = lds_frag(ap + ks * 64);
      }
    }
#pragma unroll
    for (int i = 0; i < 4; ++i) {
      if (g < 4 && i == 3) {
        aq = mfma16(a[g & 1][i], b0, aq); ak = mfma16(a[g & 1][i], b1, ak); av = mfma16(a[g & 1][i], b2, av);
      } else {
        const int ks = (g < 4) ? 3 * g + i : 12 + i;
        aq = mfma16(a[g & 1][i], wr[ks * 3], aq); ak = mfma16(a[g & 1][i], wr[ks * 3 + 1], ak); av = mfma16(a[g & 1][i], wr[ks * 3 + 2], av);
      }
    }
    asm volatile("" : "+v"(aq), "+v"(ak), "+v"(av)); __builtin_amdgcn_sched_barrier(0);
  }
}

template <class VIS>
DI void softmax_pt(const f32x4 (&st)[3], float scale, VIS vis, bf16x8& pb0, bf16x8& pb1) {
  float s[3][4]; float mx = -3.0e38f;
#pragma unroll
  for (int kt = 0; kt < 3; ++kt)
#pragma unroll
    for (int r = 0; r < 4; ++r) {
      float v = rbf(rbf(st[kt][r]) * scale);
      s[kt][r] = v;
      if (vis(kt, r)) mx = fmaxf(mx, v);
    }
  mx = red_max(mx);
  float sum = 0.f;
#pragma unroll
  for (int kt = 0; kt < 3; ++kt)
#pragma unroll
    for (int r = 0; r < 4; ++r) {
      float e = vis(kt, r) ? __expf(s[kt][r] - mx) : 0.f;
      s[kt][r] = e; sum += e;
    }
  sum = red_sum(sum);
  const float inv = 1.0f / sum;
#pragma unroll
  for (int r = 0; r < 4; ++r) {
    pb0[r] = (short)f2bf(s[0][r] * inv);
    pb0[4 + r] = (short)f2bf(s[1][r] * inv);
    pb1[r] = (short)f2bf(s[2][r] * inv);
    pb1[4 + r] = 0;
  }
}

DI void softmax_mem(const f32x4 (&st)[3], float scale, int q, int hi, bf16x8& pb0, bf16x8& pb1) {
  float s[6];
#pragma unroll
  for (int r = 0; r < 4; ++r) s[r] = hi ? st[1][r] : st[0][r];
  s[4] = hi ? st[2][2] : st[2][0];
  s[5] = hi ? st[2][3] : st[2][1];
  const bool v2 = q < 2;
  float mx = -3.0e38f;
#pragma unroll
  for (int i = 0; i < 6; ++i) { s[i] = rbf(rbf(s[i]) * scale); if (i < 4 || v2) mx = fmaxf(mx, s[i]); }
  mx = fmaxf(mx, dpp_ror<0x128>(mx));
  mx = red_max(mx);
  float sum = 0.f;
#pragma unroll
  for (int i = 0; i < 6; ++i) { const float e = (i < 4 || v2) ? __expf(s[i] - mx) : 0.f; s[i] = e; sum += e; }
  const float so = dpp_ror<0x128>(sum);
  sum = hi ? (so + sum) : (sum + so);
  sum = red_sum(sum);
  const float inv = 1.0f / sum;
  const unsigned p0 = pack2(s[0] * inv, s[1] * inv), p1 = pack2(s[2] * inv, s[3] * inv), p2 = pack2(s[4] * inv, s[5] * inv);
  const unsigned o0 = (unsigned)__builtin_amdgcn_update_dpp(0, (int)p0, 0x128, 0xf, 0xf, true);
  const unsigned o1 = (unsigned)__builtin_amdgcn_update_dpp(0, (int)p1, 0x128, 0xf, 0xf, true);
  const unsigned o2 = (unsigned)__builtin_amdgcn_update_dpp(0, (int)p2, 0x128, 0xf, 0xf, true);
  const v4u b0 = {p0, p1, o0, o1}, b1 = {p2, o2, 0u, 0u};
  pb0 = as_frag(b0); pb1 = as_frag(b1);
}

struct Params {
  const u16* ax; const u16* mem0; const u16* Wq; const u16* Wk; const u16* Wv; const u16* Wo;
  const u16* bx; const u16* wqkv; const u16* wo;
  u16* out; char* ws;
};

DI void role_s1(const Params& P, char* smem, int h) {
  int t = threadIdx.x;
  bf16x8 wr[48];
  load_w3(P.Wq, P.Wk, P.Wv, h, smem, wr, t);
  char* ws = P.ws;
  int dead = 0;
  for (int i = t; i < 8 * 16; i += 512) *(v4u*)(smem + S1_K + (40 + (i >> 4)) * KST + (i & 15) * 16) = (v4u){0, 0, 0, 0};
  for (int i = t; i < 128 * 6; i += 512) *(v2u*)(smem + S1_VT + (i / 6) * VST + 80 + (i % 6) * 8) = (v2u){0, 0};
  const float scale = 0.08838834764831845f;
  for (int s = 0; s < NSEG; ++s) {
    asm volatile("" : "+v"(t));
    const int lane = t & 63, w = t >> 6, q = lane >> 4, l15 = lane & 15;
    auto fetch_kv = [&]() {
      wait_word((const unsigned*)(ws + OFF_FLAGKV) + h * 64 + s, 1u, dead, s == 0);
      const char* kx = ws + OFF_KX + ((size_t)(s * NH + h)) * 8192;
      const char* vx = ws + OFF_VXT + ((size_t)(s * NH + h)) * 8192;
      v4u a, b;
      ld16x2_sc1(kx + t * 16, vx + t * 16, a, b);
      *(v4u*)(smem + S1_K + (8 + (t >> 4)) * KST + (t & 15) * 16) = a;
      v2u b0 = {b.x, b.y}, b1 = {b.z, b.w};
      *(v2u*)(smem + S1_VT + (t >> 2) * VST + 16 + (t & 3) * 16) = b0;
      *(v2u*)(smem + S1_VT + (t >> 2) * VST + 16 + (t & 3) * 16 + 8) = b1;
    };
    if (s > 0) fetch_kv();
    {
      const int c0 = t, c1 = 512 + t;
      v4u a, b = {0, 0, 0, 0};
      if (s == 0) {
        a = *(const v4u*)((const char*)P.mem0 + c0 * 16);
        if (w < 2) b = *(const v4u*)((const char*)P.mem0 + c1 * 16);
      } else {
        const char* src = ws + OFF_MEMBUF + (size_t)s * 10240;
        if (S1_PRE > 0) { __builtin_amdgcn_s_sleep(S1_PRE / 2); __builtin_amdgcn_s_sleep(S1_PRE - S1_PRE / 2); }
        {
          const char* prb = src + ((w * 10 + (h % 10)) * 128 + 124);
          for (int sp = 0;; ++sp) {
            unsigned pv = ld4_sc1(prb);
            if (__builtin_amdgcn_readfirstlane(pv) != SENT) break;
            if (dead || sp > SPIN_CAP) { dead = 1; break; }
          }
          if (S1_POST > 0) __builtin_amdgcn_s_sleep(S1_POST);
        }
        for (int sp = 0;; ++sp) {
          bool ok;
          if (w < 2) { ld16x2_sc1(src + c0 * 16, src + c1 * 16, a, b); ok = valid4(a) && valid4(b); }
          else { a = ld16_sc1(src + c0 * 16); ok = valid4(a); }
          if (__all(ok)) break;
          if (dead || sp > SPIN_CAP) { dead = 1; break; }
        }
      }
      if (s == 0) {
        *(v4u*)(smem + S1_A + (c0 / 80) * AST + (c0 % 80) * 16) = a;
        if (w < 2) *(v4u*)(smem + S1_A + (c1 / 80) * AST + (c1 % 80) * 16) = b;
      } else {
        *(v4u*)(smem + S1_A + ((c0 >> 1) & 7) * AST + (c0 >> 4) * 32 + (c0 & 1) * 16) = a;
        if (w < 2) *(v4u*)(smem + S1_A + ((c1 >> 1) & 7) * AST + (c1 >> 4) * 32 + (c1 & 1) * 16) = b;
      }
    }
    __syncthreads();
    f32x4 aq, ak, av;
    proj3_pipe(smem + S1_A, lane & 7, smem, wr, t, aq, ak, av);
    if (q < 2) {
#pragma unroll
      for (int r = 0; r < 4; ++r) {
        *(u16*)(smem + S1_Q + (4 * q + r) * KST + (16 * w + l15) * 2) = f2bf(aq[r]);
        *(u16*)(smem + S1_K + (4 * q + r) * KST + (16 * w + l15) * 2) = f2bf(ak[r]);
      }
      v2u vv = {pack2(av[0], av[1]), pack2(av[2], av[3])};
      *(v2u*)(smem + S1_VT + (16 * w + l15) * VST + q * 8) = vv;
    }
    if (s == 0) fetch_kv();
    __syncthreads();
    if (s < NSEG - 1 && w < 4) {
      const char* qp = smem + S1_Q + (lane & 7) * KST + q * 16;
      const char* kp = smem + S1_K + l15 * KST + q * 16;
      f32x4 st[3];
      st[0] = (f32x4){0.f, 0.f, 0.f, 0.f}; st[1] = st[0]; st[2] = st[0];
      bf16x8 qf0 = lds_frag(qp), ka0 = lds_frag(kp), kb0 = lds_frag(kp + 16 * KST), kc0 = lds_frag(kp + 32 * KST);
      bf16x8 qf1 = lds_frag(qp + 64), ka1 = lds_frag(kp + 64), kb1 = lds_frag(kp + 16 * KST + 64), kc1 = lds_frag(kp + 32 * KST + 64);
      st[0] = mfma16(ka0, qf0, st[0]); st[1] = mfma16(kb0, qf0, st[1]); st[2] = mfma16(kc0, qf0, st[2]);
      qf0 = lds_frag(qp + 128); ka0 = lds_frag(kp + 128); kb0 = lds_frag(kp + 16 * KST + 128); kc0 = lds_frag(kp + 32 * KST + 128);
      st[0] = mfma16(ka1, qf1, st[0]); st[1] = mfma16(kb1, qf1, st[1]); st[2] = mfma16(kc1, qf1, st[2]);
      qf1 = lds_frag(qp + 192); ka1 = lds_frag(kp + 192); kb1 = lds_frag(kp + 16 * KST + 192); kc1 = lds_frag(kp + 32 * KST + 192);
      st[0] = mfma16(ka0, qf0, st[0]); st[1] = mfma16(kb0, qf0, st[1]); st[2] = mfma16(kc0, qf0, st[2]);
      const char* vp = smem + S1_VT + (32 * w + l15) * VST + q * 8;
      s16x4 v0 = *(const s16x4*)(vp), v1 = *(const s16x4*)(vp + 32), v2 = *(const s16x4*)(vp + 64);
      s16x4 u0 = *(const s16x4*)(vp + 16 * VST), u1 = *(const s16x4*)(vp + 16 * VST + 32), u2 = *(const s16x4*)(vp + 16 * VST + 64);
      st[0] = mfma16(ka1, qf1, st[0]); st[1] = mfma16(kb1, qf1, st[1]); st[2] = mfma16(kc1, qf1, st[2]);
      bf16x8 pb0, pb1;
      softmax_mem(st, scale, q, l15 >> 3, pb0, pb1);
      const s16x4 z4 = {0, 0, 0, 0};
      f32x4 o = {0.f, 0.f, 0.f, 0.f}, o2 = o;
      o = mfma16(cat4(v0, v1), pb0, o);
      o2 = mfma16(cat4(u0, u1), pb0, o2);
      o = mfma16(cat4(v2, z4), pb1, o);
      o2 = mfma16(cat4(u2, z4), pb1, o2);
      {
        unsigned o00 = unsent(pack2(o[0], o[1])), o01 = unsent(pack2(o[2], o[3]));
        unsigned o10 = unsent(pack2(o2[0], o2[1])), o11 = unsent(pack2(o2[2], o2[3]));
        auto r0 = __builtin_amdgcn_permlane16_swap(o00, o00, false, false);
        auto r1 = __builtin_amdgcn_permlane16_swap(o01, o01, false, false);
        auto r2 = __builtin_amdgcn_permlane16_swap(o10, o10, false, false);
        auto r3 = __builtin_amdgcn_permlane16_swap(o11, o11, false, false);
        if (l15 < 8) {
          char* dst = ws + OFF_OBUF + (size_t)s * 65536 + h * 2048 + (2 * w + (q & 1)) * 256 + l15 * 32 + (q >> 1) * 16;
          v4u ov = (q & 1) ? (v4u){(unsigned)r2[0], (unsigned)r3[0], (unsigned)r2[1], (unsigned)r3[1]}
                           : (v4u){(unsigned)r0[0], (unsigned)r1[0], (unsigned)r0[1], (unsigned)r1[1]};
          st16_sc1(dst, ov);
        }
      }
    }
    if (t < 128) {
      v4u kv = *(const v4u*)(smem + S1_K + (t >> 4) * KST + (t & 15) * 16);
      st16_sc1(ws + OFF_KM + ((size_t)(s * NH + h)) * 2048 + t * 16, kv);
    } else if (t < 256) {
      const int d = t - 128;
      v2u a = *(const v2u*)(smem + S1_VT + d * VST), b = *(const v2u*)(smem + S1_VT + d * VST + 8);
      v4u vv = {a.x, a.y, b.x, b.y};
      st16_sc1(ws + OFF_VMT + ((size_t)(s * NH + h)) * 2048 + d * 16, vv);
    }
    drain();
    __syncthreads();
    if (t == 0) st4_sc1((unsigned*)(ws + OFF_FLAGM) + h * 64 + s, 1u);
  }
}

DI void load_wo16(const u16* __restrict__ Wo, int c, char* smem, bf16x8 (&wb)[16], int t) {
  const int lane = t & 63, w = t >> 6;
  const u16* src = Wo + (size_t)(512 * w + (lane >> 1)) * DM + 16 * c + 8 * (lane & 1);
  v4u tmp[16];
#pragma unroll
  for (int ks = 0; ks < 16; ++ks) tmp[ks] = *(const v4u*)(src + (size_t)ks * 32 * DM);
  char* base = smem + w * 16384;
#pragma unroll
  for (int ks = 0; ks < 16; ++ks) *(v4u*)(base + ks * 1024 + lane * 16) = tmp[ks];
  __syncthreads();
  const int g = lane >> 4, qq = (lane & 15) >> 2, p = lane & 3;
#pragma unroll
  for (int ks = 0; ks < 16; ++ks) {
    s16x4 lo = tr_read(base + ks * 1024 + (8 * g + qq) * 32 + p * 8);
    s16x4 hi = tr_read(base + ks * 1024 + (8 * g + 4 + qq) * 32 + p * 8);
    wb[ks] = cat4(lo, hi);
  }
  __syncthreads();
}

DI void role_s2(const Params& P, char* smem, int c, int hf) {
  const int t = threadIdx.x, lane = t & 63, w = t >> 6, q = lane >> 4, l15 = lane & 15;
  bf16x8 wb[16];
  load_wo16(P.Wo, c, smem, wb, t);
  char* ws = P.ws;
  int dead = 0;
  float* red = (float*)smem;
  for (int s = 0; s < NSEG - 1; ++s) {
    v4u a[4];
    const char* src = ws + OFF_OBUF + (size_t)s * 65536 + w * 8192 + (l15 >> 2) * 2048 + (q >> 1) * 256 + (4 * hf + (l15 & 3)) * 32 + (q & 1) * 16;
    const char* prb = ws + OFF_OBUF + (size_t)s * 65536 + w * 8192 + (c & 31) * 256 + hf * 128 + 124;
    if (S2_PRE > 0 && s > 0) { __builtin_amdgcn_s_sleep(S2_PRE / 2); __builtin_amdgcn_s_sleep(S2_PRE - S2_PRE / 2); }
    for (int sp = 0;; ++sp) {
      unsigned pv = ld4_sc1(prb);
      if (__builtin_amdgcn_readfirstlane(pv) != SENT) break;
      if (dead || sp > SPIN_CAP) { dead = 1; break; }
    }
    if (S2_POST > 0) __builtin_amdgcn_s_sleep(S2_POST);
    {
      bool ok = false;
      for (int sp = 0;; ++sp) {
        if (!ok) {
          asm volatile(
            "global_load_dwordx4 %0, %4, off sc1\n\t"
            "global_load_dwordx4 %1, %4, off offset:512 sc1\n\t"
            "global_load_dwordx4 %2, %4, off offset:1024 sc1\n\t"
            "global_load_dwordx4 %3, %4, off offset:1536 sc1\n\t"
            "s_waitcnt vmcnt(0)"
            : "=&v"(a[0]), "=&v"(a[1]), "=&v"(a[2]), "=&v"(a[3]) : "v"(src) : "memory");
          ok = valid4(a[0]) && valid4(a[1]) && valid4(a[2]) && valid4(a[3]);
        }
        if (__all(ok)) break;
        if (dead || sp > SPIN_CAP) { dead = 1; break; }
      }
    }
    f32x4 acc[4];
#pragma unroll
    for (int kq = 0; kq < 4; ++kq) acc[kq] = (f32x4){0.f, 0.f, 0.f, 0.f};
#pragma unroll
    for (int i = 0; i < 4; ++i)
#pragma unroll
      for (int kq = 0; kq < 4; ++kq) acc[kq] = mfma16(as_frag(a[i]), wb[4 * kq + i], acc[kq]);
    const f32x4 av = (q == 0) ? acc[0] : (q == 1) ? acc[1] : (q == 2) ? acc[2] : acc[3];
    float* rb = red + (s & 1) * 2048 + (w * 4 + q) * 64;
#pragma unroll
    for (int r = 0; r < 4; ++r) rb[r * 16 + l15] = av[r];
    __syncthreads();
    if (t < 256) {
      const int g = t >> 5, pi = t & 31;
      const float* rp = red + (s & 1) * 2048 + pi * 64 + g * 8;
      const f32x4 x0 = *(const f32x4*)rp, x1 = *(const f32x4*)(rp + 4);
      float y[8];
#pragma unroll
      for (int i = 0; i < 4; ++i) { y[i] = row_sum16(x0[i]); y[4 + i] = row_sum16(x1[i]); }
#pragma unroll
      for (int i = 0; i < 8; ++i) {
        unsigned u = __builtin_bit_cast(unsigned, y[i]);
        auto r = __builtin_amdgcn_permlane16_swap(u, u, false, false);
        y[i] = __builtin_bit_cast(float, (unsigned)r[0]) + __builtin_bit_cast(float, (unsigned)r[1]);
      }
      if (pi == 0) {
        v4u o = {unsent(pack2(y[0], y[1])), unsent(pack2(y[2], y[3])), unsent(pack2(y[4], y[5])), unsent(pack2(y[6], y[7]))};
        st16_sc1(ws + OFF_MEMBUF + (size_t)(s + 1) * 10240 + c * 256 + hf * 128 + g * 16, o);
      }
    }
  }
}

DI void role_s4(const Params& P, char* smem, int h, int par) {
  const int t = threadIdx.x, lane = t & 63, w = t >> 6, q = lane >> 4, l15 = lane & 15;
  char* ws = P.ws;
  int dead = 0;
  {
    bf16x8 wr[48];
    load_w3(P.Wq, P.Wk, P.Wv, h, smem, wr, t);
    v4u pa[3];
    auto fetchA = [&](int j) {
      const int row0 = (2 * (j >> 1) + par) * 32 + (j & 1) * 16;
      const char* src = (const char*)P.ax + (size_t)row0 * (DM * 2);
      pa[0] = *(const v4u*)(src + t * 16);
      pa[1] = *(const v4u*)(src + (512 + t) * 16);
      if (t < 256) pa[2] = *(const v4u*)(src + (1024 + t) * 16);
    };
    auto putA = [&](int buf) {
      char* dst = smem + S4_A + buf * 16 * AST;
      *(v4u*)(dst + (t / 80) * AST + (t % 80) * 16) = pa[0];
      *(v4u*)(dst + ((512 + t) / 80) * AST + ((512 + t) % 80) * 16) = pa[1];
      if (t < 256) *(v4u*)(dst + ((1024 + t) / 80) * AST + ((1024 + t) % 80) * 16) = pa[2];
    };
    fetchA(0); putA(0); fetchA(1);
    __syncthreads();
    for (int j = 0; j < 64; ++j) {
      const int seg = 2 * (j >> 1) + par, mt = j & 1;
      f32x4 aq, ak, av;
      proj3(smem + S4_A + (j & 1) * 16 * AST, l15, smem, wr, t, aq, ak, av);
      char* T = smem + S4_T + (j & 1) * 16 * TST;
#pragma unroll
      for (int r = 0; r < 4; ++r) {
        *(u16*)(T + (4 * q + r) * TST + (16 * w + l15) * 2) = f2bf(aq[r]);
        *(u16*)(T + (4 * q + r) * TST + 256 + (16 * w + l15) * 2) = f2bf(ak[r]);
      }
      {
        v2u vv = {pack2(av[0], av[1]), pack2(av[2], av[3])};
        st8_sc1(ws + OFF_VXT + ((size_t)(seg * NH + h)) * 8192 + (16 * w + l15) * 64 + (16 * mt + 4 * q) * 2, vv);
      }
      if (j + 1 < 64) { putA((j + 1) & 1); if (j + 2 < 64) fetchA(j + 2); }
      __syncthreads();
      {
        const int row = t >> 5, ch = t & 31;
        v4u v = *(const v4u*)(T + row * TST + ch * 16);
        const size_t o = ((size_t)(seg * NH + h)) * 8192 + (16 * mt + row) * 256 + (ch & 15) * 16;
        if (ch < 16) *(v4u*)(ws + OFF_QX + o) = v; else st16_sc1(ws + OFF_KX + o, v);
      }
      if (mt == 1) {
        drain();
        __syncthreads();
        if (t == 0) st4_sc1((unsigned*)(ws + OFF_FLAGKV) + h * 64 + seg, 1u);
      }
    }
  }
  const float scale = 0.08838834764831845f;
  const int qt = w & 1, dq = w >> 1;
  for (int i = 0; i < 32; ++i) {
    const int seg = 2 * i + par;
    wait_word((const unsigned*)(ws + OFF_FLAGM) + h * 64 + seg, 1u, dead, seg == NSEG - 1);
    const size_t sh = (size_t)(seg * NH + h);
    const char* km = ws + OFF_KM + sh * 2048;
    const char* vm = ws + OFF_VMT + sh * 2048;
    const char* kx = ws + OFF_KX + sh * 8192;
    const char* vx = ws + OFF_VXT + sh * 8192;
    const char* qx = ws + OFF_QX + sh * 8192;
    v4u kf[3][4], qf[4];
    {
      const char* p0 = (l15 < 8) ? (km + l15 * 256) : (kx + (l15 - 8) * 256);
      const char* p1 = kx + (8 + l15) * 256;
      const char* p2 = kx + (24 + (l15 & 7)) * 256;
      const char* pq = qx + (16 * qt + l15) * 256;
      v4u tmp[16];
      asm volatile(
        "global_load_dwordx4 %0, %16, off sc1\n\t"
        "global_load_dwordx4 %1, %16, off offset:64 sc1\n\t"
        "global_load_dwordx4 %2, %16, off offset:128 sc1\n\t"
        "global_load_dwordx4 %3, %16, off offset:192 sc1\n\t"
        "global_load_dwordx4 %4, %17, off sc1\n\t"
        "global_load_dwordx4 %5, %17, off offset:64 sc1\n\t"
        "global_load_dwordx4 %6, %17, off offset:128 sc1\n\t"
        "global_load_dwordx4 %7, %17, off offset:192 sc1\n\t"
        "global_load_dwordx4 %8, %18, off sc1\n\t"
        "global_load_dwordx4 %9, %18, off offset:64 sc1\n\t"
        "global_load_dwordx4 %10, %18, off offset:128 sc1\n\t"
        "global_load_dwordx4 %11, %18, off offset:192 sc1\n\t"
        "global_load_dwordx4 %12, %19, off sc1\n\t"
        "global_load_dwordx4 %13, %19, off offset:64 sc1\n\t"
        "global_load_dwordx4 %14, %19, off offset:128 sc1\n\t"
        "global_load_dwordx4 %15, %19, off offset:192 sc1\n\t"
        "s_waitcnt vmcnt(0)"
        : "=&v"(tmp[0]), "=&v"(tmp[1]), "=&v"(tmp[2]), "=&v"(tmp[3]), "=&v"(tmp[4]), "=&v"(tmp[5]), "=&v"(tmp[6]), "=&v"(tmp[7]),
          "=&v"(tmp[8]), "=&v"(tmp[9]), "=&v"(tmp[10]), "=&v"(tmp[11]), "=&v"(tmp[12]), "=&v"(tmp[13]), "=&v"(tmp[14]), "=&v"(tmp[15])
        : "v"(p0 + q * 16), "v"(p1 + q * 16), "v"(p2 + q * 16), "v"(pq + q * 16) : "memory");
#pragma unroll
      for (int ks = 0; ks < 4; ++ks) { kf[0][ks] = tmp[ks]; kf[1][ks] = tmp[4 + ks]; kf[2][ks] = tmp[8 + ks]; qf[ks] = tmp[12 + ks]; }
    }
    f32x4 st[3];
#pragma unroll
    for (int kt = 0; kt < 3; ++kt) {
      st[kt] = (f32x4){0.f, 0.f, 0.f, 0.f};
#pragma unroll
      for (int ks = 0; ks < 4; ++ks) st[kt] = mfma16(as_frag(kf[kt][ks]), as_frag(qf[ks]), st[kt]);
    }
    const int qi = 16 * qt + l15;
    bf16x8 pb0, pb1;
    softmax_pt(st, scale, [&](int kt, int r) { const int key = 16 * kt + 4 * q + r; return key < 8 || (key < 40 && key - 8 <= qi); }, pb0, pb1);
    unsigned ok[2][2];
#pragma unroll
    for (int dd = 0; dd < 2; ++dd) {
      const int d = 16 * (2 * dq + dd) + l15;
      const char* pa = (q < 2) ? (vm + d * 16 + q * 8) : (vx + d * 64 + (4 * q - 8) * 2);
      const char* pb = vx + d * 64 + (8 + 4 * q) * 2;
      const char* pc = vx + d * 64 + (24 + 4 * (q & 1)) * 2;
      v2u a, b, c;
      asm volatile("global_load_dwordx2 %0, %3, off sc1\n\tglobal_load_dwordx2 %1, %4, off sc1\n\tglobal_load_dwordx2 %2, %5, off sc1\n\ts_waitcnt vmcnt(0)"
                   : "=&v"(a), "=&v"(b), "=&v"(c) : "v"(pa), "v"(pb), "v"(pc) : "memory");
      if (q >= 2) c = (v2u){0, 0};
      v4u f0 = {a.x, a.y, b.x, b.y}, f1 = {c.x, c.y, 0, 0};
      f32x4 o = {0.f, 0.f, 0.f, 0.f};
      o = mfma16(as_frag(f0), pb0, o);
      o = mfma16(as_frag(f1), pb1, o);
      ok[dd][0] = pack2(o[0], o[1]); ok[dd][1] = pack2(o[2], o[3]);
    }
    {
      auto r0 = __builtin_amdgcn_permlane16_swap(ok[0][0], ok[0][0], false, false);
      auto r1 = __builtin_amdgcn_permlane16_swap(ok[0][1], ok[0][1], false, false);
      auto r2 = __builtin_amdgcn_permlane16_swap(ok[1][0], ok[1][0], false, false);
      auto r3 = __builtin_amdgcn_permlane16_swap(ok[1][1], ok[1][1], false, false);
      char* dst = ws + OFF_OX + ((size_t)((seg * 2 + qt) * NH + h)) * 4096 + (2 * dq + (q & 1)) * 512 + l15 * 32 + (q >> 1) * 16;
      v4u ov = (q & 1) ? (v4u){(unsigned)r2[0], (unsigned)r3[0], (unsigned)r2[1], (unsigned)r3[1]}
                       : (v4u){(unsigned)r0[0], (unsigned)r1[0], (unsigned)r0[1], (unsigned)r1[1]};
      st16_sc1(dst, ov);
    }
    drain();
    __syncthreads();
    if (t == 0) __hip_atomic_fetch_add((unsigned*)(ws + OFF_CNTOX + seg * 64), 1u, __ATOMIC_RELAXED, __HIP_MEMORY_SCOPE_AGENT);
  }
}

DI void role_s3(const Params& P, char* smem, int c, int par) {
  const int t = threadIdx.x, lane = t & 63, w = t >> 6, q = lane >> 4, l15 = lane & 15;
  bf16x8 wb0[16], wb1[16];
  load_wo16(P.Wo, 2 * c, smem, wb0, t);
  load_wo16(P.Wo, 2 * c + 1, smem, wb1, t);
  char* ws = P.ws;
  int dead = 0;
  float* red = (float*)smem;
  for (int i = 0; i < 32; ++i) {
    const int seg = 2 * i + par;
    wait_word((const unsigned*)(ws + OFF_CNTOX + seg * 64), 31u, dead, seg == NSEG - 1);
    float* rb = red + (i & 1) * 8192 + w * 1024;
#pragma unroll
    for (int mt = 0; mt < 2; ++mt) {
      f32x4 acc0 = {0.f, 0.f, 0.f, 0.f}, acc1 = acc0;
#pragma unroll
      for (int hf = 0; hf < 2; ++hf) {
        v4u a[8];
        ld16x8s1k_sc1(ws + OFF_OX + ((size_t)((seg * 2 + mt) * NH + 4 * w)) * 4096 + hf * 8192 + (q >> 1) * 512 + l15 * 32 + (q & 1) * 16, a);
#pragma unroll
        for (int ks = 0; ks < 8; ++ks) {
          acc0 = mfma16(as_frag(a[ks]), wb0[hf * 8 + ks], acc0);
          acc1 = mfma16(as_frag(a[ks]), wb1[hf * 8 + ks], acc1);
        }
        if (seg < NSEG - 1) __builtin_amdgcn_s_sleep(S3_SLEEP);
      }
#pragma unroll
      for (int r = 0; r < 4; ++r) { rb[(16 * mt + 4 * q + r) * 32 + l15] = acc0[r]; rb[(16 * mt + 4 * q + r) * 32 + 16 + l15] = acc1[r]; }
    }
    __syncthreads();
    {
      const int row = t >> 4, cp = t & 15;
      const float* rp = red + (i & 1) * 8192 + row * 32 + 2 * cp;
      float x0 = 0.f, x1 = 0.f;
#pragma unroll
      for (int ww = 0; ww < 8; ++ww) { x0 += rp[ww * 1024]; x1 += rp[ww * 1024 + 1]; }
      *(unsigned*)(P.out + (size_t)(seg * 32 + row) * OUTW + 32 * c + 2 * cp) = pack2(x0, x1);
    }
  }
}

constexpr int PB_A = 0;
constexpr int PB_R = 2 * 16 * AST;
constexpr int RST = 496;
constexpr int PB_O = PB_R + 3 * 16 * RST;
constexpr int OST = 176;
constexpr int PB_STG = 81920;
DI void role_pb(const Params& P, char* smem, int g, int rb) {
  const int t = threadIdx.x, lane = t & 63, w = t >> 6, q = lane >> 4, l15 = lane & 15;
  char* ws = P.ws;
  int dead = 0;
  const int gg = lane >> 4, qq = (lane & 15) >> 2, pp = lane & 3;
  {
    bf16x8 wb[40];
#pragma unroll
    for (int nn = 0; nn < 2; ++nn) {
      const int n = (2 * w + nn) < 15 ? (2 * w + nn) : 14;
      const int col0 = (n / 5) * DM + 80 * g + 16 * (n % 5);
      const u16* src = P.wqkv + (size_t)(lane >> 1) * 1920 + col0 + 8 * (lane & 1);
#pragma unroll
      for (int half = 0; half < 2; ++half) {
        v4u tmp[10];
#pragma unroll
        for (int i = 0; i < 10; ++i) tmp[i] = *(const v4u*)(src + (size_t)(half * 10 + i) * 32 * 1920);
        char* base = smem + PB_STG + w * 10240;
#pragma unroll
        for (int i = 0; i < 10; ++i) *(v4u*)(base + i * 1024 + lane * 16) = tmp[i];
        __syncthreads();
#pragma unroll
        for (int i = 0; i < 10; ++i) {
          s16x4 lo = tr_read(base + i * 1024 + (8 * gg + qq) * 32 + pp * 8);
          s16x4 hi = tr_read(base + i * 1024 + (8 * gg + 4 + qq) * 32 + pp * 8);
          wb[nn * 20 + half * 10 + i] = cat4(lo, hi);
        }
        __syncthreads();
      }
    }
    v4u pa[3];
    auto fetchA = [&](int j) {
      const int row0 = 512 * rb - 16 + 16 * j;
      if (row0 < 0) { pa[0] = (v4u){0, 0, 0, 0}; pa[1] = pa[0]; pa[2] = pa[0]; return; }
      const char* src = (const char*)P.bx + (size_t)row0 * (DM * 2);
      pa[0] = *(const v4u*)(src + t * 16);
      pa[1] = *(const v4u*)(src + (512 + t) * 16);
      if (t < 256) pa[2] = *(const v4u*)(src + (1024 + t) * 16);
    };
    auto putA = [&](int buf) {
      char* dst = smem + PB_A + buf * 16 * AST;
      *(v4u*)(dst + (t / 80) * AST + (t % 80) * 16) = pa[0];
      *(v4u*)(dst + ((512 + t) / 80) * AST + ((512 + t) % 80) * 16) = pa[1];
      if (t < 256) *(v4u*)(dst + ((1024 + t) / 80) * AST + ((1024 + t) % 80) * 16) = pa[2];
    };
    fetchA(0); putA(0); fetchA(1);
    __syncthreads();
    const float scale = 0.22360679774997896f;
    for (int j = 0; j < 33; ++j) {
      const char* ap = smem + PB_A + (j & 1) * 16 * AST + l15 * AST + q * 16;
      f32x4 c0 = {0.f, 0.f, 0.f, 0.f}, c1 = c0;
#pragma unroll
      for (int ks = 0; ks < 20; ++ks) {
        bf16x8 a = lds_frag(ap + ks * 64);
        c0 = mfma16(a, wb[ks], c0);
        c1 = mfma16(a, wb[20 + ks], c1);
      }
      char* R = smem + PB_R + (j % 3) * 16 * RST;
#pragma unroll
      for (int r = 0; r < 4; ++r) {
        *(u16*)(R + (4 * q + r) * RST + (32 * w + l15) * 2) = f2bf(c0[r]);
        if (w < 7) *(u16*)(R + (4 * q + r) * RST + (32 * w + 16 + l15) * 2) = f2bf(c1[r]);
      }
      if (j + 1 < 33) { putA((j + 1) & 1); if (j + 2 < 33) fetchA(j + 2); }
      __syncthreads();
      if (j >= 2 && t < 160) {
        const int row = t / 10, ch = t % 10;
        v4u v = *(const v4u*)(smem + PB_O + ((j - 1) & 1) * 16 * OST + row * OST + ch * 16);
        st16_sc1(ws + OFF_OB + (size_t)(512 * rb - 16 + 16 * (j - 1) + row) * 1280 + (80 * g) * 2 + ch * 16, v);
      }
      if (j >= 1) {
        const int item = t >> 3, wi = t & 7, row = item >> 2, hh = item & 3;
        const int srow = 512 * rb - 16 + 16 * j + row;
        const int krow = row - 2 * wi;
        const bool vis = (srow - 2 * wi) >= 0;
        const char* Rc = smem + PB_R + (j % 3) * 16 * RST;
        const char* Rp = smem + PB_R + ((j + 2) % 3) * 16 * RST;
        const char* qrow = Rc + row * RST + (20 * hh) * 2;
        const char* krowp = (krow >= 0 ? Rc + krow * RST : Rp + (krow + 16) * RST);
        float dot = 0.f;
#pragma unroll
        for (int d = 0; d < 20; d += 2) {
          unsigned qv = *(const unsigned*)(qrow + d * 2);
          unsigned kv = *(const unsigned*)(krowp + (80 + 20 * hh + d) * 2);
          dot += bf2f((u16)qv) * bf2f((u16)kv);
          dot += bf2f((u16)(qv >> 16)) * bf2f((u16)(kv >> 16));
        }
        float lg = rbf(rbf(dot) * scale);
        float mx = vis ? lg : -3.0e38f;
        mx = fmaxf(mx, __shfl_xor(mx, 1)); mx = fmaxf(mx, __shfl_xor(mx, 2)); mx = fmaxf(mx, __shfl_xor(mx, 4));
        float e = vis ? __expf(lg - mx) : 0.f;
        float sum = e;
        sum += __shfl_xor(sum, 1); sum += __shfl_xor(sum, 2); sum += __shfl_xor(sum, 4);
        const float pw = rbf(e * (1.0f / sum));
        float o0 = 0.f, o1 = 0.f, o2 = 0.f;
#pragma unroll
        for (int ww = 0; ww < 8; ++ww) {
          const float pv = __shfl(pw, (lane & ~7) + ww);
          const int kr = row - 2 * ww;
          const char* vr = (kr >= 0 ? Rc + kr * RST : Rp + (kr + 16) * RST) + (160 + 20 * hh) * 2;
          o0 += pv * bf2f(*(const u16*)(vr + wi * 2));
          o1 += pv * bf2f(*(const u16*)(vr + (wi + 8) * 2));
          if (wi < 4) o2 += pv * bf2f(*(const u16*)(vr + (wi + 16) * 2));
        }
        char* O = smem + PB_O + (j & 1) * 16 * OST + row * OST + (20 * hh) * 2;
        *(u16*)(O + wi * 2) = f2bf(o0);
        *(u16*)(O + (wi + 8) * 2) = f2bf(o1);
        if (wi < 4) *(u16*)(O + (wi + 16) * 2) = f2bf(o2);
      }
    }
    __syncthreads();
    if (t < 160) {
      const int row = t / 10, ch = t % 10;
      v4u v = *(const v4u*)(smem + PB_O + (32 & 1) * 16 * OST + row * OST + ch * 16);
      st16_sc1(ws + OFF_OB + (size_t)(512 * rb - 16 + 16 * 32 + row) * 1280 + (80 * g) * 2 + ch * 16, v);
    }
    drain();
    __syncthreads();
    if (t == 0) __hip_atomic_fetch_add((unsigned*)(ws + OFF_CNTB + rb * 64), 1u, __ATOMIC_RELAXED, __HIP_MEMORY_SCOPE_AGENT);
  }
  {
    char* stg = smem + 102400 + w * 7168;
    for (int f0 = w * 13; f0 < w * 13 + 13 && f0 < 100; f0 += 7) {
      v4u tmp[7];
#pragma unroll
      for (int i = 0; i < 7; ++i) {
        const int f = f0 + i; const bool okf = (f < w * 13 + 13) && (f < 100);
        const int ff = okf ? f : 0;
        tmp[i] = *(const v4u*)(P.wo + (size_t)((ff % 20) * 32 + (lane >> 1)) * DM + 80 * g + 16 * (ff / 20) + 8 * (lane & 1));
      }
#pragma unroll
      for (int i = 0; i < 7; ++i) *(v4u*)(stg + i * 1024 + lane * 16) = tmp[i];
      __builtin_amdgcn_s_waitcnt(0xC07F);
#pragma unroll
      for (int i = 0; i < 7; ++i) {
        const int f = f0 + i; const bool okf = (f < w * 13 + 13) && (f < 100);
        s16x4 lo = tr_read(stg + i * 1024 + (8 * gg + qq) * 32 + pp * 8);
        s16x4 hi = tr_read(stg + i * 1024 + (8 * gg + 4 + qq) * 32 + pp * 8);
        if (okf) *(bf16x8*)(smem + f * 1024 + lane * 16) = cat4(lo, hi);
      }
      __builtin_amdgcn_s_waitcnt(0xC07F);
    }
    __syncthreads();
    wait_word((const unsigned*)(ws + OFF_CNTB + rb * 64), 7u, dead);
    for (int i = 0; i < 4; ++i) {
      const int row0 = 512 * rb + 16 * (w + 8 * i);
      v4u a0[10], a1[10];
      ld16x10_sc1(ws + OFF_OB + (size_t)(row0 + l15) * 1280 + q * 16, a0);
      ld16x10_sc1(ws + OFF_OB + (size_t)(row0 + l15) * 1280 + 640 + q * 16, a1);
#pragma unroll
      for (int n = 0; n < 5; ++n) {
        f32x4 acc = {0.f, 0.f, 0.f, 0.f};
#pragma unroll
        for (int ks = 0; ks < 10; ++ks) acc = mfma16(as_frag(a0[ks]), lds_frag(smem + (n * 20 + ks) * 1024 + lane * 16), acc);
#pragma unroll
        for (int ks = 0; ks < 10; ++ks) acc = mfma16(as_frag(a1[ks]), lds_frag(smem + (n * 20 + 10 + ks) * 1024 + lane * 16), acc);
#pragma unroll
        for (int r = 0; r < 4; ++r) P.out[(size_t)(row0 + 4 * q + r) * OUTW + DM + 80 * g + 16 * n + l15] = f2bf(acc[r]);
      }
    }
  }
}

__global__ __launch_bounds__(512, 2) void fused_kernel(Params P) {
  extern __shared__ __attribute__((aligned(16))) char smem[];
  const int b = blockIdx.x;
  if (b < 32) role_s1(P, smem, b);
  else if (b < 112) role_s2(P, smem, (b - 32) % 40, (b - 32) / 40);
  else if (b < 176) role_s4(P, smem, (b - 112) & 31, (b - 112) >> 5);
  else if (b < 216) role_s3(P, smem, (b - 176) % 20, (b - 176) / 20);
  else if (b < 248) role_pb(P, smem, (b - 216) & 7, (b - 216) >> 3);
}

extern "C" void kernel_launch(void* const* d_in, const int* in_sizes, int n_in,
                              void* d_out, int out_size, void* d_ws, size_t ws_size,
                              hipStream_t stream) {
  Params P;
  P.ax = (const u16*)d_in[0]; P.mem0 = (const u16*)d_in[1];
  P.Wq = (const u16*)d_in[2]; P.Wk = (const u16*)d_in[3]; P.Wv = (const u16*)d_in[4]; P.Wo = (const u16*)d_in[5];
  P.bx = (const u16*)d_in[7]; P.wqkv = (const u16*)d_in[8]; P.wo = (const u16*)d_in[9];
  P.out = (u16*)d_out; P.ws = (char*)d_ws;
  hipFuncSetAttribute((const void*)fused_kernel, hipFuncAttributeMaxDynamicSharedMemorySize, LDS_BYTES);
  hipMemsetAsync(d_ws, 0xFF, POISON_BYTES, stream);
  fused_kernel<<<256, 512, LDS_BYTES, stream>>>(P);
}
```
